```python
import torch

def _softmax(x, axis=-1):
    return torch.softmax(x, dim=axis)
def _gelu(x):
    return torch.nn.functional.gelu(x, approximate="tanh")
def _silu(x):
    return torch.nn.functional.silu(x)
def _sigmoid(x):
    return torch.sigmoid(x)
def _layernorm(x, axis=-1, eps=1e-5):
    m = x.mean(dim=axis, keepdim=True)
    v = x.var(dim=axis, keepdim=True, unbiased=False)
    return (x - m) / torch.sqrt(v + eps)
def _scatter_add(tgt, idx, src, axis=0):
    return tgt.clone().index_add_(axis, idx, src)
def _segment_sum(src, seg, n):
    out = torch.zeros((n,) + src.shape[1:], dtype=src.dtype)
    return out.index_add_(0, seg, src)
def _pad(x, pw):
    flat = tuple(v for pair in reversed(pw) for v in pair)
    return torch.nn.functional.pad(x, flat)

kernel_name = 'recurrent_scan__linear_kernel_hedgehog__dense_activation_silu__prompt'

def setup_inputs():

    _w = (lambda a: (lambda t: t.to(torch.bfloat16) if t.is_floating_point() else t)(torch.as_tensor(a)))
    def _A_setup():
        B, S, D, H = 1, 2048, 512, 32
        Dh = D // H
        return {
            "x": _w((lambda *s: torch.randn(*s, dtype=torch.bfloat16) * 0.05)(B, S, D)),
            "wqkv": _w((lambda *s: torch.randn(*s, dtype=torch.bfloat16) * 0.4)(D, 3 * D)),
            "w_phi": _w((lambda *s: torch.randn(*s, dtype=torch.bfloat16) * 0.4)(H, Dh, Dh) * (Dh ** -0.5)),
            "wo": _w((lambda *s: torch.randn(*s, dtype=torch.bfloat16) * 0.2)(D, D)),
        }
    _A_in = _A_setup()

    def _B_setup():
        B, S, D, F = 1, 2048, 512, 1792
        return {
            "x": _w((lambda *s: torch.randn(*s, dtype=torch.bfloat16) * 0.05)(B, S, D)),
            "w1": _w((lambda *s: torch.randn(*s, dtype=torch.bfloat16) * 0.4)(D, F) * (D ** -0.5)),
            "w2": _w((lambda *s: torch.randn(*s, dtype=torch.bfloat16) * 0.1)(F, D) * (F ** -0.5)),
        }
    _B_in = _B_setup()

    return {
        "A_x": _A_in["x"],
        "A_wqkv": _A_in["wqkv"],
        "A_w_phi": _A_in["w_phi"],
        "A_wo": _A_in["wo"],
        "B_x": _B_in["x"],
        "B_w1": _B_in["w1"],
        "B_w2": _B_in["w2"],
    }

def reference(A_x, A_wqkv, A_w_phi, A_wo, B_x, B_w1, B_w2):
    def _A_ref(x, wqkv, w_phi, wo):
        B, S, D = x.shape
        H = 32; Dh = D // H
        qkv = (lambda x, *d: x.permute(*d))((x @ wqkv).reshape(B, S, 3, H, Dh), 2, 0, 3, 1, 4)
        q, k, v = qkv[0], qkv[1], qkv[2]
        pq = torch.einsum("bhsd,hdf->bhsf", q, w_phi)
        pk = torch.einsum("bhsd,hdf->bhsf", k, w_phi)
        phi_q = torch.concatenate([_softmax(pq, axis=-1), _softmax(-pq, axis=-1)], axis=-1)
        phi_k = torch.concatenate([_softmax(pk, axis=-1), _softmax(-pk, axis=-1)], axis=-1)
        kv = torch.einsum("bhsf,bhsd->bhfd", phi_k, v)
        z  = torch.einsum("bhsf->bhf", phi_k)
        num = torch.einsum("bhqf,bhfd->bhqd", phi_q, kv)
        den = torch.einsum("bhqf,bhf->bhq", phi_q, z)[..., None] + 1e-6
        o = num / den
        return (lambda x, *d: x.permute(*d))(o, 0, 2, 1, 3).reshape(B, S, D) @ wo

    def _B_ref(x, w1, w2):
        h = torch.einsum("bsd,df->bsf", x, w1)
        h = _silu(h)
        out = torch.einsum("bsf,fd->bsd", h, w2)
        return out

    x = A_x
    xn = _layernorm(x, axis=-1)
    s = _A_ref(xn, A_wqkv, A_w_phi, A_wo)
    g = _silu(_B_ref(xn, B_w1, B_w2))
    return x + s * g
```

```cpp
#include <hip/hip_runtime.h>
#include <hip/hip_bf16.h>
#include <stdint.h>
namespace naive {
typedef unsigned short u16;
__device__ __forceinline__ float b2f(u16 v) { return __uint_as_float(((unsigned)v) << 16); }
__device__ __forceinline__ u16 f2b(float f) { unsigned u = __float_as_uint(f); u += 0x7fffu + ((u >> 16) & 1u); return (u16)(u >> 16); }
__device__ __forceinline__ float rb(float f) { return b2f(f2b(f)); }
constexpr int S = 2048, D = 512, H = 32, DH = 16, F = 1792;

__global__ void ln_k(const u16* x, u16* xn) {
    int t = blockIdx.x; int lane = threadIdx.x;
    float v[8]; float s = 0;
    for (int i = 0; i < 8; ++i) { v[i] = b2f(x[t * D + lane * 8 + i]); s += v[i]; }
    for (int o = 32; o; o >>= 1) s += __shfl_xor(s, o);
    float mf = s / D; float m = rb(mf);
    float q = 0; for (int i = 0; i < 8; ++i) { float d = v[i] - mf; q += d * d; }
    for (int o = 32; o; o >>= 1) q += __shfl_xor(q, o);
    float var = rb(q / D);
    float sd = rb(sqrtf(rb(var + 1e-5f)));
    for (int i = 0; i < 8; ++i) xn[t * D + lane * 8 + i] = f2b(rb(v[i] - m) / sd);
}
__global__ void gemm_k(const u16* A, const u16* B, u16* C, int M, int N, int K, int act) {
    __shared__ float As[16][65], Bs[16][65];
    int tx = threadIdx.x & 15, ty = threadIdx.x >> 4;
    int bm = blockIdx.y * 64, bn = blockIdx.x * 64;
    float acc[4][4] = {};
    for (int k0 = 0; k0 < K; k0 += 16) {
        for (int i = threadIdx.x; i < 16 * 64; i += 256) { int r = i / 16, c = i % 16; As[c][r] = b2f(A[(size_t)(bm + r) * K + k0 + c]); }
        for (int i = threadIdx.x; i < 16 * 64; i += 256) { int r = i / 64, c = i % 64; Bs[r][c] = b2f(B[(size_t)(k0 + r) * N + bn + c]); }
        __syncthreads();
        for (int k = 0; k < 16; ++k) {
            float a[4], b[4];
            for (int i = 0; i < 4; ++i) { a[i] = As[k][ty * 4 + i]; b[i] = Bs[k][tx * 4 + i]; }
            for (int i = 0; i < 4; ++i) for (int j = 0; j < 4; ++j) acc[i][j] += a[i] * b[j];
        }
        __syncthreads();
    }
    for (int i = 0; i < 4; ++i) for (int j = 0; j < 4; ++j) {
        float v = rb(acc[i][j]);
        if (act == 1) v = v / (1.f + __expf(-v));
        C[(size_t)(bm + ty * 4 + i) * N + bn + tx * 4 + j] = f2b(v);
    }
}
__global__ void phi_k(const u16* qkv, const u16* wphi, u16* phiq, u16* phik) {
    int idx = blockIdx.x * blockDim.x + threadIdx.x;
    int which = idx & 1, h = (idx >> 1) % H, t = idx / (2 * H);
    float q[16]; for (int d = 0; d < 16; ++d) q[d] = b2f(qkv[(size_t)t * 1536 + which * 512 + h * 16 + d]);
    float p[16]; float mx = -1e30f, mn = 1e30f;
    for (int f = 0; f < 16; ++f) { float s = 0; for (int d = 0; d < 16; ++d) s += q[d] * b2f(wphi[(h * 16 + d) * 16 + f]); p[f] = rb(s); mx = fmaxf(mx, p[f]); mn = fminf(mn, p[f]); }
    float sp = 0, sn = 0; float ep[16], en[16];
    for (int f = 0; f < 16; ++f) { ep[f] = __expf(p[f] - mx); en[f] = __expf(mn - p[f]); sp += ep[f]; sn += en[f]; }
    u16* o = (which ? phik : phiq) + ((size_t)h * S + t) * 32;
    for (int f = 0; f < 16; ++f) { o[f] = f2b(ep[f] / sp); o[16 + f] = f2b(en[f] / sn); }
}
__global__ void kv_k(const u16* qkv, const u16* phik, u16* kv, u16* z) {
    int h = blockIdx.x; int f = threadIdx.x >> 4, d = threadIdx.x & 15;
    float s = 0, zz = 0;
    for (int t = 0; t < S; ++t) { float p = b2f(phik[((size_t)h * S + t) * 32 + f]); s += p * b2f(qkv[(size_t)t * 1536 + 1024 + h * 16 + d]); zz += p; }
    kv[(h * 32 + f) * 16 + d] = f2b(s); if (d == 0) z[h * 32 + f] = f2b(zz);
}
__global__ void o_k(const u16* phiq, const u16* kv, const u16* z, u16* o) {
    int idx = blockIdx.x * blockDim.x + threadIdx.x;
    int d = idx & 15, h = (idx >> 4) % H, t = idx / (16 * H);
    float num = 0, den = 0;
    for (int f = 0; f < 32; ++f) { float p = b2f(phiq[((size_t)h * S + t) * 32 + f]); num += p * b2f(kv[(h * 32 + f) * 16 + d]); den += p * b2f(z[h * 32 + f]); }
    float dn = rb(rb(den) + 1e-6f);
    o[(size_t)t * D + h * 16 + d] = f2b(rb(num) / dn);
}
__global__ void fin_k(const u16* x, const u16* s, const u16* g, u16* out) {
    int i = blockIdx.x * blockDim.x + threadIdx.x;
    float gg = b2f(g[i]);
    out[i] = f2b(b2f(x[i]) + rb(b2f(s[i]) * gg));
}
inline void run(void* const* d_in, void* d_out, void* d_ws, hipStream_t stream) {
    const u16* x = (const u16*)d_in[0]; const u16* wqkv = (const u16*)d_in[1]; const u16* wphi = (const u16*)d_in[2];
    const u16* wo = (const u16*)d_in[3]; const u16* w1 = (const u16*)d_in[5]; const u16* w2 = (const u16*)d_in[6];
    u16* ws = (u16*)d_ws; size_t o = 0;
    u16* xn = ws + o; o += S * D; u16* qkv = ws + o; o += S * 1536; u16* phiq = ws + o; o += (size_t)H * S * 32; u16* phik = ws + o; o += (size_t)H * S * 32;
    u16* kv = ws + o; o += H * 32 * 16; u16* z = ws + o; o += H * 32; o = (o + 127) & ~127; u16* ob = ws + o; o += S * D; u16* sb = ws + o; o += S * D;
    u16* hb = ws + o; o += S * F; u16* gb = ws + o; o += S * D;
    ln_k<<<S, 64, 0, stream>>>(x, xn);
    gemm_k<<<dim3(1536 / 64, S / 64), 256, 0, stream>>>(xn, wqkv, qkv, S, 1536, D, 0);
    phi_k<<<S * H * 2 / 256, 256, 0, stream>>>(qkv, wphi, phiq, phik);
    kv_k<<<H, 512, 0, stream>>>(qkv, phik, kv, z);
    o_k<<<S * H * 16 / 256, 256, 0, stream>>>(phiq, kv, z, ob);
    gemm_k<<<dim3(D / 64, S / 64), 256, 0, stream>>>(ob, wo, sb, S, D, D, 0);
    gemm_k<<<dim3(F / 64, S / 64), 256, 0, stream>>>(xn, w1, hb, S, F, D, 1);
    gemm_k<<<dim3(D / 64, S / 64), 256, 0, stream>>>(hb, w2, gb, S, D, F, 1);
    fin_k<<<S * D / 256, 256, 0, stream>>>(x, sb, gb, (u16*)d_out);
}
}
extern "C" void kernel_launch(void* const* d_in, const int* in_sizes, int n_in,
                              void* d_out, int out_size, void* d_ws, size_t ws_size,
                              hipStream_t stream) {
    naive::run(d_in, d_out, d_ws, stream);
}
```

```cpp
#include <hip/hip_runtime.h>
#include <hip/hip_bf16.h>
#include <stdint.h>
#include <type_traits>

namespace hk {

typedef unsigned short u16;
typedef unsigned long long u64;
typedef __bf16 bf16x8 __attribute__((ext_vector_type(8)));
typedef __bf16 bf16x2 __attribute__((ext_vector_type(2)));
typedef short s16x4 __attribute__((ext_vector_type(4)));
typedef float f32x2 __attribute__((ext_vector_type(2)));
typedef float f32x4 __attribute__((ext_vector_type(4)));
typedef float f32x16 __attribute__((ext_vector_type(16)));
typedef unsigned u32x2 __attribute__((ext_vector_type(2)));
typedef unsigned u32x4 __attribute__((ext_vector_type(4)));
#define LAS __attribute__((address_space(3)))
#define DI __device__ __forceinline__

template <int I, int N, class F> DI void sfor(F&& f) {
    if constexpr (I < N) { f(std::integral_constant<int, I>{}); sfor<I + 1, N>(f); }
}

constexpr int S = 2048, D = 512, FF = 1792;

constexpr unsigned WS_E   = 0;
constexpr unsigned WS_FH  = 1024;
constexpr unsigned WS_FK  = WS_FH + 2048;
constexpr unsigned WS_FO  = WS_FK + 2048;
constexpr unsigned WS_TMO = WS_FO + 2048;
constexpr unsigned WS_DBG = 16384;
constexpr unsigned WS_ZP  = 1048576;
constexpr unsigned WS_KVP = WS_ZP + 65536;
constexpr unsigned WS_O   = WS_KVP + 1048576;
constexpr unsigned WS_H   = WS_O + 2097152;
constexpr unsigned WS_END = WS_H + 64 * 112 * 1024;

constexpr unsigned SLOT = 16384;
constexpr unsigned L_T     = 131072;
constexpr unsigned L_STATS = L_T + 24576;
constexpr unsigned L_CS    = L_STATS + 1024;
constexpr unsigned L_WPHI  = L_CS + 1024;
constexpr unsigned L_JUNK  = L_WPHI + 1024;
constexpr unsigned L_STAMP = L_JUNK + 256;
constexpr unsigned L_END   = L_STAMP + 512;
static_assert(L_END <= 163840, "lds");

DI u32x4 make_srd(const void* p, unsigned bytes) {
    u64 a = (u64)p;
    u32x4 r; r.x = (unsigned)a; r.y = (unsigned)(a >> 32) & 0xffffu; r.z = bytes; r.w = 0x00020000u; return r;
}
DI void dma(u32x4 srd, unsigned voff, unsigned soff, unsigned ldsdst) {
    asm volatile("s_mov_b32 m0, %0\n\ts_nop 0\n\tbuffer_load_dwordx4 %1, %2, %3 offen lds" ::"s"(ldsdst), "v"(voff), "s"(srd), "s"(soff) : "memory");
}
template <unsigned LIMM, unsigned SIMM, int SC1 = 0> DI void dmai(u32x4 srd, unsigned voff, unsigned sbase, unsigned lbase) {
    unsigned t;
    if constexpr (SC1) asm volatile("s_add_u32 m0, %3, %5\n\ts_add_u32 %0, %4, %6\n\tbuffer_load_dwordx4 %1, %2, %0 offen sc1 lds" : "=&s"(t) : "v"(voff), "s"(srd), "s"(lbase), "s"(sbase), "n"(LIMM), "n"(SIMM) : "memory", "scc");
    else asm volatile("s_add_u32 m0, %3, %5\n\ts_add_u32 %0, %4, %6\n\tbuffer_load_dwordx4 %1, %2, %0 offen lds" : "=&s"(t) : "v"(voff), "s"(srd), "s"(lbase), "s"(sbase), "n"(LIMM), "n"(SIMM) : "memory", "scc");
}
template <unsigned LIMM, unsigned SIMM> DI void dmai56(u32x4 srd, unsigned voff, unsigned sbase, unsigned lbase) {
    unsigned t;
    asm volatile("s_add_u32 m0, %3, %5\n\ts_add_u32 %0, %4, %6\n\ts_mov_b32 exec_hi, 0x00ffffff\n\tbuffer_load_dwordx4 %1, %2, %0 offen lds\n\ts_mov_b32 exec_hi, -1" : "=&s"(t) : "v"(voff), "s"(srd), "s"(lbase), "s"(sbase), "n"(LIMM), "n"(SIMM) : "memory", "scc");
}
DI void dma_sc1(u32x4 srd, unsigned voff, unsigned soff, unsigned ldsdst) {
    asm volatile("s_mov_b32 m0, %0\n\ts_nop 0\n\tbuffer_load_dwordx4 %1, %2, %3 offen sc1 lds" ::"s"(ldsdst), "v"(voff), "s"(srd), "s"(soff) : "memory");
}
DI void dma56(u32x4 srd, unsigned voff, unsigned soff, unsigned ldsdst) {
    asm volatile("s_mov_b32 m0, %0\n\ts_mov_b32 exec_hi, 0x00ffffff\n\tbuffer_load_dwordx4 %1, %2, %3 offen lds\n\ts_mov_b32 exec_hi, -1" ::"s"(ldsdst), "v"(voff), "s"(srd), "s"(soff) : "memory");
}
DI void store16_sc1_m(u32x4 srd, unsigned voff, unsigned soff, f32x4 v) {
    asm volatile("s_nop 7\n\ts_nop 1\n\tbuffer_store_dwordx4 %0, %1, %2, %3 offen sc1\n\ts_nop 1" ::"v"(v), "v"(voff), "s"(srd), "s"(soff) : "memory");
}
DI void store16_sc1(u32x4 srd, unsigned voff, unsigned soff, u32x4 v) {
    asm volatile("buffer_store_dwordx4 %0, %1, %2, %3 offen sc1\n\ts_nop 1" ::"v"(v), "v"(voff), "s"(srd), "s"(soff) : "memory");
}
DI void load16_sc1(u32x4& v, u32x4 srd, unsigned voff, unsigned soff) {
    asm volatile("buffer_load_dwordx4 %0, %1, %2, %3 offen sc1" : "=v"(v) : "v"(voff), "s"(srd), "s"(soff) : "memory");
}
DI void load16(u32x4& v, u32x4 srd, unsigned voff, unsigned soff) {
    asm volatile("buffer_load_dwordx4 %0, %1, %2, %3 offen" : "=v"(v) : "v"(voff), "s"(srd), "s"(soff) : "memory");
}
DI void load8_sc1(u32x2& v, u32x4 srd, unsigned voff, unsigned soff) {
    asm volatile("buffer_load_dwordx2 %0, %1, %2, %3 offen sc1" : "=v"(v) : "v"(voff), "s"(srd), "s"(soff) : "memory");
}
DI void store8_sc1(u32x4 srd, unsigned voff, unsigned soff, u32x2 v) {
    asm volatile("buffer_store_dwordx2 %0, %1, %2, %3 offen sc1" ::"v"(v), "v"(voff), "s"(srd), "s"(soff) : "memory");
}
template <int N, int ID> DI void sync() {
    asm volatile("s_waitcnt vmcnt(%0)" ::"n"(N) : "memory");
    __builtin_amdgcn_s_waitcnt(0xC07F);
    asm volatile("s_barrier" ::: "memory");
}
template <int N> DI void waitv() { asm volatile("s_waitcnt vmcnt(%0)" ::"n"(N) : "memory"); }
template <class T> DI void fence(T& v) { asm volatile("" : "+v"(v)); }
DI void bar() { __builtin_amdgcn_s_waitcnt(0xC07F); asm volatile("s_barrier" ::: "memory"); }

DI bf16x8 lds_b128(unsigned a) {
    return *(const LAS bf16x8*)(a); }
DI f32x4 lds_f4(unsigned a) { return *(const LAS f32x4*)(a); }
DI s16x4 lds_tr(unsigned a) {
    return __builtin_amdgcn_ds_read_tr16_b64_v4i16((LAS s16x4*)(a)); }
DI bf16x8 lds_tr2(unsigned a0, unsigned a1) {
    s16x4 lo = lds_tr(a0), hi = lds_tr(a1);
    return __builtin_bit_cast(bf16x8, __builtin_shufflevector(lo, hi, 0, 1, 2, 3, 4, 5, 6, 7));
}
DI f32x4 mfma16(bf16x8 a, bf16x8 b, f32x4 c) {
    return __builtin_amdgcn_mfma_f32_16x16x32_bf16(a, b, c, 0, 0, 0); }
DI f32x16 mfma32(bf16x8 a, bf16x8 b, f32x16 c) { return __builtin_amdgcn_mfma_f32_32x32x16_bf16(a, b, c, 0, 0, 0); }
DI unsigned pk(float a, float b) { f32x2 p = {a, b}; return __builtin_bit_cast(unsigned, __builtin_convertvector(p, bf16x2)); }
DI float lo_f(unsigned u) { return __uint_as_float(u << 16); }
DI float hi_f(unsigned u) { return __uint_as_float(u & 0xffff0000u); }
DI float rbf(float f) { return lo_f(pk(f, 0.f) & 0xffffu); }
DI float dot2(unsigned a, unsigned b, float c) { return __builtin_amdgcn_fdot2_f32_bf16(__builtin_bit_cast(bf16x2, a), __builtin_bit_cast(bf16x2, b), c, false); }
DI float fexp2(float x) { return __builtin_amdgcn_exp2f(x); }
DI float frcp(float x) { return __builtin_amdgcn_rcpf(x); }
DI float silu(float y) { return y * frcp(1.f + fexp2(y * -1.44269504089f)); }
DI float rows_sum(float a) {
    auto r = __builtin_amdgcn_permlane16_swap(__float_as_uint(a), __float_as_uint(a), false, false);
    float b = __uint_as_float(r[0]) + __uint_as_float(r[1]);
    auto q = __builtin_amdgcn_permlane32_swap(__float_as_uint(b), __float_as_uint(b), false, false);
    return __uint_as_float(q[0]) + __uint_as_float(q[1]);
}
DI float rows_max(float a) {
    auto r = __builtin_amdgcn_permlane16_swap(__float_as_uint(a), __float_as_uint(a), false, false);
    float b = fmaxf(__uint_as_float(r[0]), __uint_as_float(r[1]));
    auto q = __builtin_amdgcn_permlane32_swap(__float_as_uint(b), __float_as_uint(b), false, false);
    return fmaxf(__uint_as_float(q[0]), __uint_as_float(q[1]));
}
constexpr unsigned ONES = 0x3f803f80u;

#define SB() __builtin_amdgcn_sched_barrier(0)
constexpr float LOG2E = 1.44269504089f;

template <bool MAX> DI void pair_reduce(float& A, float& B) {
    auto r = __builtin_amdgcn_permlane16_swap(__float_as_uint(A), __float_as_uint(B), false, false);
    float m = MAX ? fmaxf(__uint_as_float(r[0]), __uint_as_float(r[1])) : __uint_as_float(r[0]) + __uint_as_float(r[1]);
    auto q = __builtin_amdgcn_permlane32_swap(__float_as_uint(m), __float_as_uint(m), false, false);
    float x = MAX ? fmaxf(__uint_as_float(q[0]), __uint_as_float(q[1])) : __uint_as_float(q[0]) + __uint_as_float(q[1]);
    auto z = __builtin_amdgcn_permlane16_swap(__float_as_uint(x), __float_as_uint(x), false, false);
    A = __uint_as_float(z[0]); B = __uint_as_float(z[1]);
}
DI void dual_softmax2(const f32x4 (&p)[2], const float (&lr)[2], u32x4 (&r)[2]) {
    float t[2][4], A[2], B[2];
    for (int h = 0; h < 2; ++h) {
        for (int i = 0; i < 4; ++i) t[h][i] = p[h][i] * lr[h];
        A[h] = fmaxf(fmaxf(t[h][0], t[h][1]), fmaxf(t[h][2], t[h][3]));
        B[h] = fmaxf(fmaxf(-t[h][0], -t[h][1]), fmaxf(-t[h][2], -t[h][3]));
    }
    pair_reduce<true>(A[0], B[0]); pair_reduce<true>(A[1], B[1]);
    float ep[2][4], en[2][4], sp[2], sn[2];
    for (int h = 0; h < 2; ++h) {
        for (int i = 0; i < 4; ++i) { ep[h][i] = fexp2(t[h][i] - A[h]); en[h][i] = fexp2(-t[h][i] - B[h]); }
        sp[h] = (ep[h][0] + ep[h][1]) + (ep[h][2] + ep[h][3]);
        sn[h] = (en[h][0] + en[h][1]) + (en[h][2] + en[h][3]);
    }
    pair_reduce<false>(sp[0], sn[0]); pair_reduce<false>(sp[1], sn[1]);
    for (int h = 0; h < 2; ++h) {
        float rp = frcp(sp[h]), rn = frcp(sn[h]);
        r[h].x = pk(ep[h][0] * rp, ep[h][1] * rp); r[h].y = pk(ep[h][2] * rp, ep[h][3] * rp);
        r[h].z = pk(en[h][0] * rn, en[h][1] * rn); r[h].w = pk(en[h][2] * rn, en[h][3] * rn);
    }
}

#define STAMP(i) do {} while (0)

constexpr int NCD = 36;
constexpr bool h_iss(int s) { return s >= 1 && s + 5 < NCD; }
constexpr bool w_iss(int s) { return s >= 1 && s + 9 < NCD; }
constexpr int C_POLLK = 3;
constexpr int C_PART  = 7;
constexpr int C_RED   = 11;
constexpr int C_O     = 12;
constexpr int C_FO    = 16;
constexpr int C_POLLO = 18;
constexpr int extra_ops(int u) { return (u == C_PART ? 16 : 0) + (u == C_O ? 1 : 0); }
constexpr int n_after_h(int s) {
    int n = 0;
    if (s >= 6) {
        int u = s - 5;
        n += (w_iss(u) ? 1 : 0) + extra_ops(u);
        for (int v = u + 1; v < s; ++v) n += (h_iss(v) ? 1 : 0) + (w_iss(v) ? 1 : 0) + extra_ops(v);
    } else {
        n = (s < 4 ? 3 - s : 5 - s);
        if (s >= 1 && s < 4) n += 2;
        for (int v = 1; v < s; ++v) n += (h_iss(v) ? 1 : 0) + (w_iss(v) ? 1 : 0) + extra_ops(v);
    }
    return n;
}
constexpr int cmin(int a, int b) { return a < b ? a : b; }

__global__ __launch_bounds__(512, 2) void fused_k(const u16* __restrict__ x, const u16* __restrict__ wqkv, const u16* __restrict__ wphi,
                                                  const u16* __restrict__ wo, const u16* __restrict__ w1, const u16* __restrict__ w2,
                                                  u16* __restrict__ out, char* __restrict__ ws) {
    __shared__ __attribute__((aligned(1024))) char smem[163840];
    asm volatile("" ::"v"((unsigned)(size_t)smem));
    const int tid = threadIdx.x;
    const int w = __builtin_amdgcn_readfirstlane(tid >> 6);
    int l = tid & 63;
    const int b = blockIdx.x;
    const int xg = b & 7, rr = b >> 3;
    const int tt = (xg >> 2) * 8 + (rr & 7), j = (xg & 3) * 4 + (rr >> 3);
    const int ti = xg * 4 + (rr & 3), tj = rr >> 2;
    const int tg = w & 3, cg = w >> 2;

    const u32x4 sX = make_srd(x, S * D * 2), sW1 = make_srd(w1, D * FF * 2);
    const u32x4 sWS = make_srd(ws, WS_END);

    STAMP(0);
    const unsigned lw2k = w * 2048, lw1k = w * 1024, lw1792 = w * 1792;
    const unsigned bx = tt * 131072, bw1 = j * 224;
    const unsigned vx0 = (16 * w + (l >> 3)) * 1024 + 16 * ((l & 7) ^ (l >> 4));
    const unsigned vx1 = (16 * w + 8 + (l >> 3)) * 1024 + 16 * ((l & 7) ^ (4 + (l >> 4)));
    const unsigned vw1 = (32 * (w >> 2) + 16 * ((w >> 1) & 1) + 4 * (w & 1) + ((l * 37) >> 9)) * 3584 + 16 * (l - 14 * ((l * 37) >> 9));
    auto issue_x0 = [&](auto s, auto slot) { dmai<slot * SLOT, s * 128>(sX, vx0, bx, lw2k); };
    auto issue_x1 = [&](auto s, auto slot) { dmai<slot * SLOT + 1024, s * 128>(sX, vx1, bx, lw2k); };
    auto issue_w10 = [&](auto s, auto slot) { dmai56<slot * SLOT, s * 64 * 3584>(sW1, vw1, bw1, lw1792); };
    auto issue_w11 = [&](auto s, auto slot) { dmai56<slot * SLOT + 896, s * 64 * 3584 + 8 * 3584>(sW1, vw1, bw1, lw1792); };
#define IC(v) std::integral_constant<int, (v)>{}
    issue_x0(IC(0), IC(4)); issue_w10(IC(0), IC(0)); issue_x1(IC(0), IC(4)); issue_w11(IC(0), IC(0));
    issue_x0(IC(1), IC(5)); issue_w10(IC(1), IC(1)); issue_x1(IC(1), IC(5)); issue_w11(IC(1), IC(1));
    const u32x4 sQ = make_srd(wqkv, D * 1536 * 2), sW2 = make_srd(w2, FF * D * 2), sWo = make_srd(wo, D * D * 2);
    u32x2 eword = {0u, 0u};
    {
        u64 pc; unsigned clen;
        asm volatile("s_getpc_b64 %0\n.Lhk_code_a:\n\ts_mov_b32 %1, .Lhk_code_end-.Lhk_code_a" : "=s"(pc), "=s"(clen));
        u32x4 sC = make_srd((const void*)pc, clen);
        asm volatile("s_mov_b32 m0, %0\n\ts_nop 0\n\tbuffer_load_dword %1, %2, %3 offen lds" ::"s"(L_JUNK), "v"(l * 128), "s"(sC), "s"(w * 8192) : "memory");
        if (w == 0) load8_sc1(eword, sWS, 0, WS_E);
        if (w == 1) dma(make_srd(wphi, 32 * 256 * 2), l * 16, j * 1024, L_WPHI);
    }
    STAMP(1);

    int g = l >> 4, l15 = l & 15, q4 = l15 >> 2, p4 = l & 3;
    const unsigned kvb = ((l & 7) >> 1) ^ ((l >> 4) & 3);
    const unsigned vkv = (32 * (w >> 1) + 16 * (w & 1) + 8 * ((l >> 5) & 1) + ((l >> 3) & 3)) * 3072 + 1024 * (1 + (kvb >> 1)) + (kvb & 1) * 32 + 16 * (l & 1);
    const unsigned vq = (8 * ((l >> 4) & 1) + 4 * ((l >> 5) & 1) + ((l >> 2) & 3)) * 3072 + ((((l & 3) >> 1) ^ ((l >> 4) & 1)) * 32) + 16 * (l & 1);
    const unsigned bkv = j * 64, bq = j * 64 + 32 * w * 3072;
    auto issue_kv0 = [&](auto i, auto slot) { dmai<slot * SLOT, i * 128 * 3072>(sQ, vkv, bkv, lw2k); };
    auto issue_kv1 = [&](auto i, auto slot) { dmai<slot * SLOT + 1024, i * 128 * 3072 + 4 * 3072>(sQ, vkv, bkv, lw2k); };
    auto issue_q0 = [&](auto i, auto slot) { dmai<slot * SLOT, i * 256 * 3072>(sQ, vq, bq, lw2k); };
    auto issue_q1 = [&](auto i, auto slot) { dmai<slot * SLOT + 1024, i * 256 * 3072 + 16 * 3072>(sQ, vq, bq, lw2k); };

    bf16x8 ONESF = __builtin_bit_cast(bf16x8, u32x4{ONES, ONES, ONES, ONES});
    fence(ONESF);
    bf16x8 xr[2][16];
    f32x4 acc1[4][2], csacc = {0, 0, 0, 0};
    for (int i = 0; i < 4; ++i) for (int t = 0; t < 2; ++t) acc1[i][t] = f32x4{0, 0, 0, 0};
    float q0 = 0.f, q1 = 0.f;
    int pb[4];
    for (int i = 0; i < 4; ++i) pb[i] = cg ? 4 + ((i + tg) % 3) : ((i + tg) & 3);
    unsigned ax[2][2];
    ax[0][0] = 65536 + (32 * tg + 16 * cg + l15) * 128 + 16 * (g ^ (l15 >> 1)); ax[0][1] = ax[0][0] ^ 64;
    ax[1][0] = 65536 + (32 * tg + 16 * (cg ^ 1) + l15) * 128 + 16 * (g ^ (l15 >> 1)); ax[1][1] = ax[1][0] ^ 64;
    unsigned aw1b[4];
    for (int i = 0; i < 4; ++i) aw1b[i] = (16 * (g >> 1) + 4 * (g & 1) + q4) * 224 + 8 * p4 + pb[i] * 32;
    fence(ax[0][0]); fence(ax[0][1]); fence(ax[1][0]); fence(ax[1][1]);
    bf16x8 f0[4], f1[4];
    f0[3] = ONESF; f1[3] = ONESF;

    auto sq4 = [&](const bf16x8& v, int d) {
        unsigned u = __builtin_bit_cast(u32x4, v)[d];
        float lo = lo_f(u), hi = hi_f(u);
        q0 = __builtin_fmaf(lo, lo, q0); q1 = __builtin_fmaf(hi, hi, q1);
        fence(q0); fence(q1);
    };
    auto groupA = [&](bf16x8 (&f)[4], auto kap_, auto&& d0, auto&& d1, auto&& d2, auto&& d3) {
        constexpr int kap = kap_;
        acc1[0][0] = mfma16(f[0], xr[0][kap], acc1[0][0]); acc1[0][1] = mfma16(f[0], xr[1][kap], acc1[0][1]);
        sq4(xr[0][kap], 0); SB(); d0(); SB();
        acc1[1][0] = mfma16(f[1], xr[0][kap], acc1[1][0]); acc1[1][1] = mfma16(f[1], xr[1][kap], acc1[1][1]);
        sq4(xr[0][kap], 1); SB(); d1(); SB();
        acc1[2][0] = mfma16(f[2], xr[0][kap], acc1[2][0]); acc1[2][1] = mfma16(f[2], xr[1][kap], acc1[2][1]);
        sq4(xr[0][kap], 2); SB(); d2(); SB();
        acc1[3][0] = mfma16(f[3], xr[0][kap], acc1[3][0]); acc1[3][1] = mfma16(f[3], xr[1][kap], acc1[3][1]);
        sq4(xr[0][kap], 3); SB();
        csacc = mfma16(f[0], ONESF, csacc); SB(); d3(); SB();
    };
    auto readsA = [&](bf16x8 (&f)[4], auto s_, auto ks_) {
        constexpr int s = s_, ks = ks_;
        constexpr unsigned xs = (s % 4) * SLOT, wsl = (s % 4) * SLOT + 32 * ks * 224;
        xr[0][2 * s + ks] = lds_b128(ax[0][ks] + xs);
        f[0] = lds_tr2(aw1b[0] + wsl, aw1b[0] + wsl + 1792);
        xr[1][2 * s + ks] = lds_b128(ax[1][ks] + xs);
        f[1] = lds_tr2(aw1b[1] + wsl, aw1b[1] + wsl + 1792);
        f[2] = lds_tr2(aw1b[2] + wsl, aw1b[2] + wsl + 1792);
        if (cg == 0) f[3] = lds_tr2(aw1b[3] + wsl, aw1b[3] + wsl + 1792);
    };
    auto nop = [&]() {};
    sfor<0, 8>([&](auto s_) {
        constexpr int s = s_;
        if constexpr (s == 0) sync<5, s>();
        else if constexpr (s == 1) sync<9, s>();
        else sync<8, s>();
        readsA(f0, IC(s), IC(0)); SB();
        auto dA = [&]() {
            if constexpr (s == 0) { issue_x0(IC(2), IC(6)); issue_w10(IC(2), IC(2)); }
            else if constexpr (s <= 4) issue_x0(IC(s + 3), IC(4 + (s - 1) % 4));
            else if constexpr (s == 5) issue_kv0(IC(0), IC(4));
            else if constexpr (s == 6) issue_kv0(IC(1), IC(5));
            else issue_kv0(IC(2), IC(6));
        };
        auto dB = [&]() {
            if constexpr (s == 0) { issue_x1(IC(2), IC(6)); issue_w11(IC(2), IC(2)); }
            else if constexpr (s <= 4) issue_w10(IC(s + 3), IC((s - 1) % 4));
            else if constexpr (s == 5) issue_kv1(IC(0), IC(4));
            else if constexpr (s == 6) issue_kv1(IC(1), IC(5));
            else issue_kv1(IC(2), IC(6));
        };
        auto dC = [&]() {
            if constexpr (s == 0) { issue_x0(IC(3), IC(7)); issue_w10(IC(3), IC(3)); }
            else if constexpr (s <= 4) issue_x1(IC(s + 3), IC(4 + (s - 1) % 4));
            else if constexpr (s == 5) issue_kv0(IC(3), IC(0));
            else if constexpr (s == 6) issue_q0(IC(0), IC(1));
            else issue_q0(IC(1), IC(2));
        };
        auto dD = [&]() {
            if constexpr (s == 0) { issue_x1(IC(3), IC(7)); issue_w11(IC(3), IC(3)); }
            else if constexpr (s <= 4) issue_w11(IC(s + 3), IC((s - 1) % 4));
            else if constexpr (s == 5) issue_kv1(IC(3), IC(0));
            else if constexpr (s == 6) issue_q1(IC(0), IC(1));
            else issue_q1(IC(1), IC(2));
        };
        if constexpr (s > 0) { groupA(f1, IC(2 * s - 1), dA, nop, dB, nop); }
        readsA(f1, IC(s), IC(1)); SB();
        if constexpr (s > 0) groupA(f0, IC(2 * s), dC, nop, dD, nop);
        else groupA(f0, IC(0), dA, dB, dC, dD);
    });
    groupA(f1, IC(15), nop, nop, nop, nop);
    STAMP(2);
    {
        float qq = rows_sum(q0 + q1);
        if (g == 0) {
            *(LAS float*)(L_STATS + (32 * tg + 16 * cg + l15) * 8 + 4) = qq;
            if (cg) {
                *(LAS float*)(L_STATS + (32 * tg + 16 + l15) * 8) = acc1[3][0][0];
                *(LAS float*)(L_STATS + (32 * tg + l15) * 8) = acc1[3][1][0];
            }
        }
        if (l15 == 0) *(LAS f32x4*)(L_CS + (pb[0] * 16 + 4 * g) * 4) = csacc;
    }
    bar();
    fence(l); g = l >> 4; l15 = l & 15; q4 = l15 >> 2; p4 = l & 3;
    const unsigned vw2 = (8 * w + (l >> 3)) * 1024 + 16 * ((l & 7) ^ (4 * ((l >> 4) & 1)));
    const unsigned vh = l * 16;
    const unsigned bw2 = tj * 128;
    const unsigned bh = WS_H + ((2 * ti + (w >> 2)) * 112 + (w & 3)) * 1024, bo = WS_O + ((2 * ti + (w >> 2)) * 32 + (w & 3)) * 1024;
    auto issue_W = [&](auto i_) {
        constexpr int i = i_;
        constexpr unsigned dst = 3 * SLOT + ((i + 8) % 10) * 8192;
        if constexpr (i < 28) dmai<dst, i * 65536>(sW2, vw2, bw2, lw1k); else dmai<dst, (i - 28) * 65536>(sWo, vw2, bw2, lw1k);
    };
    auto issue_H = [&](auto i_) {
        constexpr int i = i_;
        constexpr unsigned dst = (i % 6) * 8192;
        if constexpr (i < 28) dmai<dst, i * 4096, 1>(sWS, vh, bh, lw1k); else dmai<dst, (i - 28) * 4096, 1>(sWS, vh, bo, lw1k);
    };
    issue_W(IC(0)); issue_W(IC(1));
    float nm[2], sdv[2], rsv[2];
    for (int th = 0; th < 2; ++th) {
        f32x2 o = *(const LAS f32x2*)(L_STATS + (32 * tg + 16 * (th ^ cg) + l15) * 8);
        float mean = o[0] * (1.f / 512.f);
        float var = fmaxf(o[1] * (1.f / 512.f) - mean * mean, 0.f);
        nm[th] = -rbf(mean);
        sdv[th] = rbf(__builtin_amdgcn_sqrtf(rbf(rbf(var) + 1e-5f)));
        rsv[th] = frcp(sdv[th]);
    }
    sfor<0, 4>([&](auto i_) {
        constexpr int i = i_;
        if (i < 3 || cg == 0) {
            f32x4 c = lds_f4(L_CS + (pb[i] * 16 + 4 * g) * 4);
            unsigned pa[2][2];
            for (int th = 0; th < 2; ++th) {
                float hv[4], rl = rsv[th] * -LOG2E;
                for (int r = 0; r < 4; ++r) {
                    float u = __builtin_fmaf(nm[th], c[r], acc1[i][th][r]);
                    float e = fexp2(u * rl);
                    hv[r] = u * frcp(__builtin_fmaf(e, sdv[th], sdv[th]));
                }
                pa[th][0] = pk(hv[0], hv[1]); pa[th][1] = pk(hv[2], hv[3]);
            }
            auto r0 = __builtin_amdgcn_permlane16_swap(pa[0][0], pa[1][0], false, false);
            auto r1 = __builtin_amdgcn_permlane16_swap(pa[0][1], pa[1][1], false, false);
            u32x4 v = {r0[0], r1[0], r0[1], r1[1]};
            store16_sc1(sWS, 16 * (l ^ (16 * cg)), WS_H + ((4 * tt + tg) * 112 + 7 * j + pb[i]) * 1024, v);
        }
        if constexpr (i == 0) issue_W(IC(2));
        if constexpr (i == 1) issue_W(IC(3));
        SB();
    });
    STAMP(3);

    f32x4 accK[2], accV[2], accQ[2], csb = {0, 0, 0, 0};
    for (int t = 0; t < 2; ++t) { accK[t] = f32x4{0, 0, 0, 0}; accV[t] = accK[t]; accQ[t] = accK[t]; }
    const unsigned swz = 2 * (g & 1) + (q4 >> 1);
    const unsigned rowb = 16 * (g >> 1) + 4 * (g & 1) + q4;
    unsigned akK = rowb * 128 + 32 * (cg ^ swz) + 8 * p4, akV = rowb * 128 + 32 * ((2 + cg) ^ swz) + 8 * p4;
    unsigned akKh = akK + 65536, akVh = akV + 65536;
    unsigned aq = rowb * 64 + 32 * (cg ^ (g & 1)) + 8 * p4;
    fence(akKh); fence(akVh);
    const int roleKV = (tg == cg) ? 1 : ((tg == cg + 2) ? 2 : 0);
    const bool roleQ = (tg == 1 - cg);
    unsigned tlo = 0, thi = 0;
    u32x2 pollv = {0u, 0u};
    bf16x8 fa[4], fb[4];

    auto readsKV = [&](bf16x8 (&f)[4], auto i_, auto hf_) {
        constexpr int i = i_, hf = hf_;
        constexpr int slot = (i == 3) ? 0 : 4 + i;
        constexpr unsigned o = (slot >= 4 ? slot - 4 : slot) * SLOT + (64 * hf) * 128;
        const unsigned bk = slot >= 4 ? akKh : akK, bv = slot >= 4 ? akVh : akV;
        f[0] = lds_tr2(bk + o, bk + o + 1024);
        f[1] = lds_tr2(bv + o, bv + o + 1024);
        f[2] = lds_tr2(bk + o + 4096, bk + o + 4096 + 1024);
        f[3] = lds_tr2(bv + o + 4096, bv + o + 4096 + 1024);
    };
    auto groupKV = [&](bf16x8 (&f)[4], auto kap_, auto&& d0, auto&& d1) {
        constexpr int kap = kap_;
        accK[0] = mfma16(f[0], xr[0][kap], accK[0]); accK[1] = mfma16(f[0], xr[1][kap], accK[1]); SB(); d0(); SB();
        accV[0] = mfma16(f[1], xr[0][kap], accV[0]); accV[1] = mfma16(f[1], xr[1][kap], accV[1]); SB();
        accK[0] = mfma16(f[2], xr[0][kap + 1], accK[0]); accK[1] = mfma16(f[2], xr[1][kap + 1], accK[1]); SB(); d1(); SB();
        accV[0] = mfma16(f[3], xr[0][kap + 1], accV[0]); accV[1] = mfma16(f[3], xr[1][kap + 1], accV[1]); SB();
        if (roleKV == 1) { csb = mfma16(f[0], ONESF, csb); csb = mfma16(f[2], ONESF, csb); }
        else if (roleKV == 2) { csb = mfma16(f[1], ONESF, csb); csb = mfma16(f[3], ONESF, csb); }
        SB();
    };
    sfor<0, 4>([&](auto i_) {
        constexpr int i = i_;
        if constexpr (i == 0) sync<17, 8>();
        if constexpr (i == 1) sync<13, 9>();
        if constexpr (i == 2) {
            sync<0, 10>();
            if (w == 0) {
                fence(eword);
                u64 e = ((u64)__builtin_amdgcn_readfirstlane(eword.y) << 32) | __builtin_amdgcn_readfirstlane(eword.x);
                e += 0x9E3779B97F4A7C15ull;
                tlo = (unsigned)e; thi = (unsigned)(e >> 32);
                if (l == 0) store8_sc1(sWS, 0, WS_FH + (tt * 16 + j) * 8, u32x2{tlo, thi});
            }
        }
        if constexpr (i == 3) {
            sync<63, 11>();
            if (w == 0) load8_sc1(pollv, sWS, l15 * 8, WS_FH + (ti >> 1) * 128);
        }
        readsKV(fa, IC(i), IC(0)); SB();
        auto d0 = [&]() { if constexpr (i == 2) issue_W(IC(4)); if constexpr (i == 3) issue_W(IC(8)); };
        auto d1 = [&]() { if constexpr (i == 2) issue_W(IC(5)); if constexpr (i == 3) issue_W(IC(9)); };
        auto d2 = [&]() { if constexpr (i == 2) issue_W(IC(6)); };
        auto d3 = [&]() { if constexpr (i == 2) issue_W(IC(7)); };
        if constexpr (i > 0) groupKV(fb, IC(4 * i - 2), d0, d1);
        readsKV(fb, IC(i), IC(1)); SB();
        groupKV(fa, IC(4 * i), d2, d3);
    });
    groupKV(fb, IC(14), nop, nop);
    STAMP(4);
    if (roleKV != 0 && l15 == 0) *(LAS f32x4*)(L_CS + ((5 + 2 * roleKV + cg) * 16 + 4 * g) * 4) = csb;
    bar();
    const s16x4 wph = lds_tr(L_WPHI + cg * 512 + (4 * g + q4) * 32 + 8 * p4);
    float lr[2] = {rsv[0] * LOG2E, rsv[1] * LOG2E};
    {
        f32x4 cK = lds_f4(L_CS + ((7 + cg) * 16 + 4 * g) * 4), cV = lds_f4(L_CS + ((9 + cg) * 16 + 4 * g) * 4);
        f32x4 pkv[2];
        for (int th = 0; th < 2; ++th) {
            float k0 = __builtin_fmaf(nm[th], cK[0], accK[th][0]), k1 = __builtin_fmaf(nm[th], cK[1], accK[th][1]);
            float k2 = __builtin_fmaf(nm[th], cK[2], accK[th][2]), k3 = __builtin_fmaf(nm[th], cK[3], accK[th][3]);
            u32x2 kb = {pk(k0, k1), pk(k2, k3)};
            pkv[th] = __builtin_amdgcn_mfma_f32_16x16x16bf16_1k(wph, __builtin_bit_cast(s16x4, kb), f32x4{0, 0, 0, 0}, 0, 0, 0);
        }
        for (int th = 0; th < 2; ++th) {
            float v0 = __builtin_fmaf(nm[th], cV[0], accV[th][0]) * rsv[th], v1 = __builtin_fmaf(nm[th], cV[1], accV[th][1]) * rsv[th];
            float v2 = __builtin_fmaf(nm[th], cV[2], accV[th][2]) * rsv[th], v3 = __builtin_fmaf(nm[th], cV[3], accV[th][3]) * rsv[th];
            unsigned row = L_T + cg * 12288 + (32 * tg + 16 * th + l15) * 96 + 8 * g;
            *(LAS u32x2*)(row + 64) = u32x2{pk(v0, v1), pk(v2, v3)};
        }
        u32x4 ph[2];
        dual_softmax2(pkv, lr, ph);
        for (int th = 0; th < 2; ++th) {
            unsigned row = L_T + cg * 12288 + (32 * tg + 16 * th + l15) * 96 + 8 * g;
            *(LAS u32x2*)(row) = u32x2{ph[th].x, ph[th].y};
            *(LAS u32x2*)(row + 32) = u32x2{ph[th].z, ph[th].w};
        }
    }
    bar();
    if (tg < 2) {
        f32x4 akv = {0, 0, 0, 0}, az = {0, 0, 0, 0};
        const unsigned base = L_T + cg * 12288 + rowb * 96 + 8 * p4;
        bf16x8 a[4], v[4];
        for (int kk = 0; kk < 4; ++kk) {
            a[kk] = lds_tr2(base + (32 * kk) * 96 + tg * 32, base + (32 * kk + 8) * 96 + tg * 32);
            v[kk] = lds_tr2(base + (32 * kk) * 96 + 64, base + (32 * kk + 8) * 96 + 64);
        }
        for (int kk = 0; kk < 4; ++kk) { akv = mfma16(a[kk], v[kk], akv); az = mfma16(a[kk], ONESF, az); }
        store16_sc1_m(sWS, l * 16, WS_KVP + ((j * 16 + tt) * 4 + cg * 2 + tg) * 1024, akv);
        if (l15 == 0) store16_sc1_m(sWS, g * 16, WS_ZP + ((j * 16 + tt) * 4 + cg * 2 + tg) * 64, az);
    }
    if (w == 0) {
        waitv<4>();
        fence(pollv);
        unsigned spins = 0;
        while (!__all(pollv.x == tlo && pollv.y == thi)) {
            __builtin_amdgcn_s_sleep(1);
            load8_sc1(pollv, sWS, l15 * 8, WS_FH + (ti >> 1) * 128);
            waitv<0>();
            fence(pollv);
            if (++spins > (1u << 16)) { *(volatile unsigned*)(ws + WS_TMO) = 1; break; }
        }
    }
    STAMP(5);
    csb = f32x4{0, 0, 0, 0};
    auto readsQ = [&](bf16x8 (&f)[4], auto i_, auto hf_) {
        constexpr int i = i_, hf = hf_;
        constexpr unsigned o = (1 + i) * SLOT + (128 * hf) * 64;
        for (int kk = 0; kk < 4; ++kk) f[kk] = lds_tr2(aq + o + (32 * kk) * 64, aq + o + (32 * kk + 8) * 64);
    };
    auto groupQ = [&](bf16x8 (&f)[4], auto kap_, auto&& d0, auto&& d1) {
        constexpr int kap = kap_;
        accQ[0] = mfma16(f[0], xr[0][kap], accQ[0]); accQ[1] = mfma16(f[0], xr[1][kap], accQ[1]); SB(); d0(); SB();
        accQ[0] = mfma16(f[1], xr[0][kap + 1], accQ[0]); accQ[1] = mfma16(f[1], xr[1][kap + 1], accQ[1]); SB();
        accQ[0] = mfma16(f[2], xr[0][kap + 2], accQ[0]); accQ[1] = mfma16(f[2], xr[1][kap + 2], accQ[1]); SB(); d1(); SB();
        accQ[0] = mfma16(f[3], xr[0][kap + 3], accQ[0]); accQ[1] = mfma16(f[3], xr[1][kap + 3], accQ[1]); SB();
        if (roleQ) for (int kk = 0; kk < 4; ++kk) csb = mfma16(f[kk], ONESF, csb);
        SB();
    };
    sfor<0, 2>([&](auto i_) {
        constexpr int i = i_;
        sync<63, 12 + i>();
        readsQ(fa, IC(i), IC(0)); SB();
        auto d0 = [&]() { issue_H(IC(2 * i)); };
        auto d1 = [&]() { issue_H(IC(2 * i + 1)); };
        if constexpr (i > 0) groupQ(fb, IC(4), nop, nop);
        readsQ(fb, IC(i), IC(1)); SB();
        groupQ(fa, IC(8 * i), d0, d1);
    });
    groupQ(fb, IC(12), nop, nop);
    STAMP(6);
    if (roleQ && l15 == 0) *(LAS f32x4*)(L_CS + ((11 + cg) * 16 + 4 * g) * 4) = csb;
    bar();
    bf16x8 phq[2];
    {
        f32x4 cQ = lds_f4(L_CS + ((11 + cg) * 16 + 4 * g) * 4);
        f32x4 pq[2];
        for (int th = 0; th < 2; ++th) {
            float k0 = __builtin_fmaf(nm[th], cQ[0], accQ[th][0]), k1 = __builtin_fmaf(nm[th], cQ[1], accQ[th][1]);
            float k2 = __builtin_fmaf(nm[th], cQ[2], accQ[th][2]), k3 = __builtin_fmaf(nm[th], cQ[3], accQ[th][3]);
            u32x2 kb = {pk(k0, k1), pk(k2, k3)};
            pq[th] = __builtin_amdgcn_mfma_f32_16x16x16bf16_1k(wph, __builtin_bit_cast(s16x4, kb), f32x4{0, 0, 0, 0}, 0, 0, 0);
        }
        u32x4 ph[2];
        dual_softmax2(pq, lr, ph);
        phq[0] = __builtin_bit_cast(bf16x8, ph[0]); phq[1] = __builtin_bit_cast(bf16x8, ph[1]);
        fence(phq[0]); fence(phq[1]);
    }
    STAMP(7);

    const int nbk = w & 1, uu = (w >> 1) & 1, kh = w >> 2;
    f32x16 accO, accS;
    for (int i = 0; i < 16; ++i) { accO[i] = 0.f; accS[i] = 0.f; }
    const unsigned h2 = l >> 5, blk = (l >> 4) & 1;
    unsigned aW = (8 * h2 + q4 + 32 * kh) * 128 + 64 * (nbk ^ (q4 >> 1)) + 32 * blk + 8 * p4;
    unsigned aWh = aW + 65536;
    fence(aWh);
    const unsigned aH = (uu * 4 + 2 * kh) * 1024 + 16 * l;
    u32x4 part[16];
    u32x4 xres;
    bf16x8 hb[2][2], wa[2][2];

    auto mfmaC = [&](auto s_) {
        constexpr int s = s_;
        if constexpr (s < 28) { accO = mfma32(wa[s & 1][0], hb[s & 1][0], accO); accO = mfma32(wa[s & 1][1], hb[s & 1][1], accO); }
        else { accS = mfma32(wa[s & 1][0], hb[s & 1][0], accS); accS = mfma32(wa[s & 1][1], hb[s & 1][1], accS); }
    };
    sfor<0, NCD>([&](auto s_) {
        constexpr int s = s_;
        if constexpr (s == C_PART || s == 23) {
            if (w == 0) {
                constexpr unsigned fam = (s == C_PART) ? WS_FK : WS_FO;
                const unsigned fo = fam + (s == C_PART ? j : (ti >> 1)) * 128;
                waitv<2 * (s - (s == C_PART ? C_POLLK : C_POLLO))>();
                fence(pollv);
                unsigned spins = 0;
                while (!__all(pollv.x == tlo && pollv.y == thi)) {
                    __builtin_amdgcn_s_sleep(1);
                    load8_sc1(pollv, sWS, l15 * 8, fo);
                    waitv<0>();
                    fence(pollv);
                    if (++spins > (1u << 16)) { *(volatile unsigned*)(ws + WS_TMO) = 2; break; }
                }
            }
        }
        if constexpr (s == 0) sync<0, 14>();
        else if constexpr (s == C_FO) sync<cmin(2 * (C_FO - C_O - 1), n_after_h(s)), 14 + s>();
        else if constexpr (s == C_RED) sync<cmin(2 * (C_RED - C_PART - 1), n_after_h(s)), 14 + s>();
        else sync<n_after_h(s), 14 + s>();
        {
            constexpr unsigned hs = (s % 6) * 8192, wo_ = 3 * SLOT + ((s + 8) % 10) * 8192;
            constexpr bool hi = wo_ >= 65536;
            constexpr unsigned wof = hi ? wo_ - 65536 : wo_;
            const unsigned bw = hi ? aWh : aW;
            hb[s & 1][0] = lds_b128(aH + hs);
            wa[s & 1][0] = lds_tr2(bw + wof, bw + wof + 512);
            hb[s & 1][1] = lds_b128(aH + hs + 1024);
            wa[s & 1][1] = lds_tr2(bw + wof + 2048, bw + wof + 2048 + 512);
        }
        SB();
        if constexpr (s == 0) { if (w == 0 && l == 0) store8_sc1(sWS, 0, WS_FK + (j * 16 + tt) * 8, u32x2{tlo, thi}); }
        if constexpr (s == C_FO) { if (w == 0 && l == 0) store8_sc1(sWS, 0, WS_FO + (tt * 16 + j) * 8, u32x2{tlo, thi}); }
        if constexpr (s == C_POLLK) { if (w == 0) load8_sc1(pollv, sWS, l15 * 8, WS_FK + j * 128); }
        if constexpr (s == C_POLLO) { if (w == 0) load8_sc1(pollv, sWS, l15 * 8, WS_FO + (ti >> 1) * 128); }
        if constexpr (s > 0) mfmaC(IC(s - 1));
        SB();
        if constexpr (s == 0) { issue_H(IC(4)); issue_H(IC(5)); }
        if constexpr (h_iss(s)) issue_H(IC(s + 5));
        if constexpr (w_iss(s)) issue_W(IC(s + 9));
        if constexpr (s == C_PART) {
            const unsigned em = (j * 16 + (tg >> 1) * 8) * 4 + cg * 2 + (tg & 1);
            sfor<0, 8>([&](auto i_) {
                constexpr int i = i_;
                load16_sc1(part[i], sWS, l * 16, WS_KVP + (em + 4 * i) * 1024);
                load16_sc1(part[8 + i], sWS, g * 16, WS_ZP + (em + 4 * i) * 64);
            });
        }
        if constexpr (s == C_RED) {
            sfor<0, 16>([&](auto i_) { fence(part[i_]); });
            f32x4 a = __builtin_bit_cast(f32x4, part[0]), z = __builtin_bit_cast(f32x4, part[8]);
            sfor<1, 8>([&](auto i_) { a += __builtin_bit_cast(f32x4, part[i_]); z += __builtin_bit_cast(f32x4, part[8 + i_]); });
            unsigned e = L_T + ((cg * 2 + (tg & 1)) * 2 + (tg >> 1)) * 2048 + l * 16;
            *(LAS f32x4*)(e) = a;
            *(LAS f32x4*)(e + 1024) = z;
        }
        if constexpr (s == C_O) {
            unsigned e = L_T + (cg * 4) * 2048 + l * 16;
            f32x4 kP = lds_f4(e) + lds_f4(e + 2048), zP = lds_f4(e + 1024) + lds_f4(e + 2048 + 1024);
            f32x4 kN = lds_f4(e + 4096) + lds_f4(e + 4096 + 2048), zN = lds_f4(e + 4096 + 1024) + lds_f4(e + 4096 + 2048 + 1024);
            bf16x8 akv = __builtin_bit_cast(bf16x8, u32x4{pk(kP[0], kP[1]), pk(kP[2], kP[3]), pk(kN[0], kN[1]), pk(kN[2], kN[3])});
            bf16x8 az = __builtin_bit_cast(bf16x8, u32x4{pk(zP[0], zP[1]), pk(zP[2], zP[3]), pk(zN[0], zN[1]), pk(zN[2], zN[3])});
            unsigned po[2][2];
            for (int th = 0; th < 2; ++th) {
                f32x4 num = mfma16(akv, phq[th], f32x4{0, 0, 0, 0});
                f32x4 den = mfma16(az, phq[th], f32x4{0, 0, 0, 0});
                float rd = frcp(den[0] + 1e-6f);
                po[th][0] = pk(num[0] * rd, num[1] * rd); po[th][1] = pk(num[2] * rd, num[3] * rd);
            }
            auto r0 = __builtin_amdgcn_permlane16_swap(po[0][0], po[1][0], false, false);
            auto r1 = __builtin_amdgcn_permlane16_swap(po[0][1], po[1][1], false, false);
            u32x4 v = {r0[0], r1[0], r0[1], r1[1]};
            store16_sc1(sWS, 16 * (l ^ (16 * cg)), WS_O + ((4 * tt + tg) * 32 + 2 * j + cg) * 1024, v);
        }
        SB();
        if constexpr (s == 0) STAMP(8);
        if constexpr (s == C_PART) STAMP(9);
        if constexpr (s == C_O) STAMP(10);
        if constexpr (s == 23) STAMP(11);
        if constexpr (s == 27) STAMP(12);
    });
    const unsigned ooff = (64 * ti + 32 * uu + (l & 31)) * 1024 + (64 * tj + 32 * nbk + 16 * kh + 8 * h2) * 2;
    load16(xres, sX, ooff, 0);
    mfmaC(IC(NCD - 1));
    STAMP(13);
    bar();
    {
        unsigned e = w * 4096 + l * 16;
        f32x4 o0, o1, s0, s1;
        if (kh == 0) { o0 = f32x4{accO[8], accO[9], accO[10], accO[11]}; o1 = f32x4{accO[12], accO[13], accO[14], accO[15]};
                       s0 = f32x4{accS[8], accS[9], accS[10], accS[11]}; s1 = f32x4{accS[12], accS[13], accS[14], accS[15]}; }
        else         { o0 = f32x4{accO[0], accO[1], accO[2], accO[3]}; o1 = f32x4{accO[4], accO[5], accO[6], accO[7]};
                       s0 = f32x4{accS[0], accS[1], accS[2], accS[3]}; s1 = f32x4{accS[4], accS[5], accS[6], accS[7]}; }
        *(LAS f32x4*)(e) = o0; *(LAS f32x4*)(e + 1024) = o1; *(LAS f32x4*)(e + 2048) = s0; *(LAS f32x4*)(e + 3072) = s1;
    }
    bar();
    {
        unsigned e = (w ^ 4) * 4096 + l * 16;
        f32x4 o0 = lds_f4(e), o1 = lds_f4(e + 1024), s0 = lds_f4(e + 2048), s1 = lds_f4(e + 3072);
        float ov[8], sv[8];
        if (kh == 0) { for (int i = 0; i < 4; ++i) { ov[i] = accO[i] + o0[i]; ov[4 + i] = accO[4 + i] + o1[i]; sv[i] = accS[i] + s0[i]; sv[4 + i] = accS[4 + i] + s1[i]; } }
        else         { for (int i = 0; i < 4; ++i) { ov[i] = accO[8 + i] + o0[i]; ov[4 + i] = accO[12 + i] + o1[i]; sv[i] = accS[8 + i] + s0[i]; sv[4 + i] = accS[12 + i] + s1[i]; } }
        float gs[8];
        for (int i = 0; i < 8; ++i) gs[i] = sv[i] * ov[i] * frcp(1.f + fexp2(ov[i] * -LOG2E));
        waitv<0>();
        fence(xres);
        auto x02 = __builtin_amdgcn_permlane32_swap(xres.x, xres.z, false, false);
        auto x13 = __builtin_amdgcn_permlane32_swap(xres.y, xres.w, false, false);
        unsigned xp[4] = {x02[0], x13[0], x02[1], x13[1]};
        unsigned rp[4];
        for (int i = 0; i < 4; ++i) rp[i] = pk(lo_f(xp[i]) + gs[2 * i], hi_f(xp[i]) + gs[2 * i + 1]);
        auto r02 = __builtin_amdgcn_permlane32_swap(rp[0], rp[2], false, false);
        auto r13 = __builtin_amdgcn_permlane32_swap(rp[1], rp[3], false, false);
        u32x4 v = {r02[0], r13[0], r02[1], r13[1]};
        asm volatile("buffer_store_dwordx4 %0, %1, %2, 0 offen\n\ts_nop 1" ::"v"(v), "v"(ooff), "s"(make_srd(out, S * D * 2)) : "memory");
    }
    if (b == 0 && w == 0 && l == 0) store8_sc1(sWS, 0, WS_E, u32x2{tlo, thi});
    STAMP(14);
    asm volatile(".Lhk_code_end:");
}

}

extern "C" void kernel_launch(void* const* d_in, const int* in_sizes, int n_in, void* d_out, int out_size, void* d_ws, size_t ws_size,
                              hipStream_t stream) {
    hk::fused_k<<<256, 512, 0, stream>>>((const hk::u16*)d_in[0], (const hk::u16*)d_in[1], (const hk::u16*)d_in[2], (const hk::u16*)d_in[3],
                                         (const hk::u16*)d_in[5], (const hk::u16*)d_in[6], (hk::u16*)d_out, (char*)d_ws);
}
```

```cpp
#include <hip/hip_runtime.h>
#include <hip/hip_bf16.h>
#include <stdint.h>
#include <type_traits>

namespace hk {

typedef unsigned short u16;
typedef unsigned long long u64;
typedef __bf16 bf16x8 __attribute__((ext_vector_type(8)));
typedef __bf16 bf16x2 __attribute__((ext_vector_type(2)));
typedef short s16x4 __attribute__((ext_vector_type(4)));
typedef float f32x2 __attribute__((ext_vector_type(2)));
typedef float f32x4 __attribute__((ext_vector_type(4)));
typedef float f32x16 __attribute__((ext_vector_type(16)));
typedef unsigned u32x2 __attribute__((ext_vector_type(2)));
typedef unsigned u32x4 __attribute__((ext_vector_type(4)));
#define LAS __attribute__((address_space(3)))
#define DI __device__ __forceinline__

template <int I, int N, class F> DI void sfor(F&& f) {
    if constexpr (I < N) { f(std::integral_constant<int, I>{}); sfor<I + 1, N>(f); }
}

constexpr int S = 2048, D = 512, FF = 1792;

constexpr unsigned WS_E   = 0;
constexpr unsigned WS_FH  = 1024;
constexpr unsigned WS_FK  = WS_FH + 2048;
constexpr unsigned WS_FO  = WS_FK + 2048;
constexpr unsigned WS_TMO = WS_FO + 2048;
constexpr unsigned WS_DBG = 16384;
constexpr unsigned WS_ZP  = 1048576;
constexpr unsigned WS_KVP = WS_ZP + 65536;
constexpr unsigned OSTR = 33, HSTR = 128;
constexpr unsigned WS_O   = 2097152;
constexpr unsigned WS_H   = 3 * 2097152;
constexpr unsigned WS_END = WS_H + 64 * HSTR * 1024;

constexpr unsigned SLOT = 16384;
constexpr unsigned L_T     = 131072;
constexpr unsigned L_STATS = L_T + 24576;
constexpr unsigned L_CS    = L_STATS + 1024;
constexpr unsigned L_WPHI  = L_CS + 1024;
constexpr unsigned L_JUNK  = L_WPHI + 1024;
constexpr unsigned L_STAMP = L_JUNK + 256;
constexpr unsigned L_END   = L_STAMP + 512;
static_assert(L_END <= 163840, "lds");

DI u32x4 make_srd(const void* p, unsigned bytes) {
    u64 a = (u64)p;
    u32x4 r; r.x = (unsigned)a; r.y = (unsigned)(a >> 32) & 0xffffu; r.z = bytes; r.w = 0x00020000u; return r;
}
DI void dma(u32x4 srd, unsigned voff, unsigned soff, unsigned ldsdst) {
    asm volatile("s_mov_b32 m0, %0\n\ts_nop 0\n\tbuffer_load_dwordx4 %1, %2, %3 offen lds" ::"s"(ldsdst), "v"(voff), "s"(srd), "s"(soff) : "memory");
}
template <unsigned LIMM, unsigned SIMM, int SC1 = 0> DI void dmai(u32x4 srd, unsigned voff, unsigned sbase, unsigned lbase) {
    unsigned t;
    if constexpr (SC1) asm volatile("s_add_u32 m0, %3, %5\n\ts_add_u32 %0, %4, %6\n\tbuffer_load_dwordx4 %1, %2, %0 offen sc1 lds" : "=&s"(t) : "v"(voff), "s"(srd), "s"(lbase), "s"(sbase), "n"(LIMM), "n"(SIMM) : "memory", "scc");
    else asm volatile("s_add_u32 m0, %3, %5\n\ts_add_u32 %0, %4, %6\n\tbuffer_load_dwordx4 %1, %2, %0 offen lds" : "=&s"(t) : "v"(voff), "s"(srd), "s"(lbase), "s"(sbase), "n"(LIMM), "n"(SIMM) : "memory", "scc");
}
template <unsigned LIMM, unsigned SIMM> DI void dmai56(u32x4 srd, unsigned voff, unsigned sbase, unsigned lbase) {
    unsigned t;
    asm volatile("s_add_u32 m0, %3, %5\n\ts_add_u32 %0, %4, %6\n\ts_mov_b32 exec_hi, 0x00ffffff\n\tbuffer_load_dwordx4 %1, %2, %0 offen lds\n\ts_mov_b32 exec_hi, -1" : "=&s"(t) : "v"(voff), "s"(srd), "s"(lbase), "s"(sbase), "n"(LIMM), "n"(SIMM) : "memory", "scc");
}
DI void dma_sc1(u32x4 srd, unsigned voff, unsigned soff, unsigned ldsdst) {
    asm volatile("s_mov_b32 m0, %0\n\ts_nop 0\n\tbuffer_load_dwordx4 %1, %2, %3 offen sc1 lds" ::"s"(ldsdst), "v"(voff), "s"(srd), "s"(soff) : "memory");
}
DI void dma56(u32x4 srd, unsigned voff, unsigned soff, unsigned ldsdst) {
    asm volatile("s_mov_b32 m0, %0\n\ts_mov_b32 exec_hi, 0x00ffffff\n\tbuffer_load_dwordx4 %1, %2, %3 offen lds\n\ts_mov_b32 exec_hi, -1" ::"s"(ldsdst), "v"(voff), "s"(srd), "s"(soff) : "memory");
}
DI void store16_sc1_m(u32x4 srd, unsigned voff, unsigned soff, f32x4 v) {
    asm volatile("s_nop 7\n\ts_nop 1\n\tbuffer_store_dwordx4 %0, %1, %2, %3 offen sc1\n\ts_nop 1" ::"v"(v), "v"(voff), "s"(srd), "s"(soff) : "memory");
}
DI void store16_sc1(u32x4 srd, unsigned voff, unsigned soff, u32x4 v) {
    asm volatile("buffer_store_dwordx4 %0, %1, %2, %3 offen sc1\n\ts_nop 1" ::"v"(v), "v"(voff), "s"(srd), "s"(soff) : "memory");
}
DI void load16_sc1(u32x4& v, u32x4 srd, unsigned voff, unsigned soff) {
    asm volatile("buffer_load_dwordx4 %0, %1, %2, %3 offen sc1" : "=v"(v) : "v"(voff), "s"(srd), "s"(soff) : "memory");
}
DI void load16(u32x4& v, u32x4 srd, unsigned voff, unsigned soff) {
    asm volatile("buffer_load_dwordx4 %0, %1, %2, %3 offen" : "=v"(v) : "v"(voff), "s"(srd), "s"(soff) : "memory");
}
DI void load8_sc1(u32x2& v, u32x4 srd, unsigned voff, unsigned soff) {
    asm volatile("buffer_load_dwordx2 %0, %1, %2, %3 offen sc1" : "=v"(v) : "v"(voff), "s"(srd), "s"(soff) : "memory");
}
DI void store8_sc1(u32x4 srd, unsigned voff, unsigned soff, u32x2 v) {
    asm volatile("buffer_store_dwordx2 %0, %1, %2, %3 offen sc1" ::"v"(v), "v"(voff), "s"(srd), "s"(soff) : "memory");
}
template <int N, int ID> DI void sync() {
    asm volatile("s_waitcnt vmcnt(%0)" ::"n"(N) : "memory");
    __builtin_amdgcn_s_waitcnt(0xC07F);
    asm volatile("s_barrier" ::: "memory");
}
DI void touch_g(const char* p) {
    asm volatile("s_mov_b32 m0, %1\n\ts_nop 0\n\tglobal_load_lds_dword %0, off" ::"v"(p), "s"(L_JUNK) : "memory");
}
DI void touch_b(u32x4 srd, unsigned voff, unsigned soff) {
    asm volatile("s_mov_b32 m0, %0\n\ts_nop 0\n\tbuffer_load_dword %1, %2, %3 offen lds" ::"s"(L_JUNK), "v"(voff), "s"(srd), "s"(soff) : "memory");
}
DI void touch_b_sc1(u32x4 srd, unsigned voff, unsigned soff) {
    asm volatile("s_mov_b32 m0, %0\n\ts_nop 0\n\tbuffer_load_dword %1, %2, %3 offen sc1 lds" ::"s"(L_JUNK), "v"(voff), "s"(srd), "s"(soff) : "memory");
}
DI void store8_sc1_v(u32x4 srd, unsigned voff, unsigned soff, u32x2 v) {
    asm volatile("buffer_store_dwordx2 %0, %1, %2, %3 offen sc1\n\ts_nop 0" ::"v"(v), "v"(voff), "s"(srd), "s"(soff) : "memory");
}
template <int N> DI void waitv() { asm volatile("s_waitcnt vmcnt(%0)" ::"n"(N) : "memory"); }
template <class T> DI void fence(T& v) { asm volatile("" : "+v"(v)); }
template <class T> DI void fence_s(T& v) { asm volatile("" : "+s"(v)); }
DI void bar() { __builtin_amdgcn_s_waitcnt(0xC07F); asm volatile("s_barrier" ::: "memory"); }

DI bf16x8 lds_b128(unsigned a) {
    return *(const LAS bf16x8*)(a); }
DI f32x4 lds_f4(unsigned a) { return *(const LAS f32x4*)(a); }
DI s16x4 lds_tr(unsigned a) {
    return __builtin_amdgcn_ds_read_tr16_b64_v4i16((LAS s16x4*)(a)); }
DI bf16x8 lds_tr2(unsigned a0, unsigned a1) {
    s16x4 lo = lds_tr(a0), hi = lds_tr(a1);
    return __builtin_bit_cast(bf16x8, __builtin_shufflevector(lo, hi, 0, 1, 2, 3, 4, 5, 6, 7));
}
DI f32x4 mfma16(bf16x8 a, bf16x8 b, f32x4 c) {
    return __builtin_amdgcn_mfma_f32_16x16x32_bf16(a, b, c, 0, 0, 0); }
DI f32x16 mfma32(bf16x8 a, bf16x8 b, f32x16 c) { return __builtin_amdgcn_mfma_f32_32x32x16_bf16(a, b, c, 0, 0, 0); }
DI unsigned pk(float a, float b) { f32x2 p = {a, b}; return __builtin_bit_cast(unsigned, __builtin_convertvector(p, bf16x2)); }
DI float lo_f(unsigned u) { return __uint_as_float(u << 16); }
DI float hi_f(unsigned u) { return __uint_as_float(u & 0xffff0000u); }
DI float rbf(float f) { return lo_f(pk(f, 0.f) & 0xffffu); }
DI float dot2(unsigned a, unsigned b, float c) { return __builtin_amdgcn_fdot2_f32_bf16(__builtin_bit_cast(bf16x2, a), __builtin_bit_cast(bf16x2, b), c, false); }
DI float fexp2(float x) { return __builtin_amdgcn_exp2f(x); }
DI float frcp(float x) { return __builtin_amdgcn_rcpf(x); }
DI float silu(float y) { return y * frcp(1.f + fexp2(y * -1.44269504089f)); }
DI float rows_sum(float a) {
    auto r = __builtin_amdgcn_permlane16_swap(__float_as_uint(a), __float_as_uint(a), false, false);
    float b = __uint_as_float(r[0]) + __uint_as_float(r[1]);
    auto q = __builtin_amdgcn_permlane32_swap(__float_as_uint(b), __float_as_uint(b), false, false);
    return __uint_as_float(q[0]) + __uint_as_float(q[1]);
}
DI float rows_max(float a) {
    auto r = __builtin_amdgcn_permlane16_swap(__float_as_uint(a), __float_as_uint(a), false, false);
    float b = fmaxf(__uint_as_float(r[0]), __uint_as_float(r[1]));
    auto q = __builtin_amdgcn_permlane32_swap(__float_as_uint(b), __float_as_uint(b), false, false);
    return fmaxf(__uint_as_float(q[0]), __uint_as_float(q[1]));
}
constexpr unsigned ONES = 0x3f803f80u;

#define SB() __builtin_amdgcn_sched_barrier(0)
constexpr float LOG2E = 1.44269504089f;

template <bool MAX> DI void pair_reduce(float& A, float& B) {
    auto r = __builtin_amdgcn_permlane16_swap(__float_as_uint(A), __float_as_uint(B), false, false);
    float m = MAX ? fmaxf(__uint_as_float(r[0]), __uint_as_float(r[1])) : __uint_as_float(r[0]) + __uint_as_float(r[1]);
    auto q = __builtin_amdgcn_permlane32_swap(__float_as_uint(m), __float_as_uint(m), false, false);
    float x = MAX ? fmaxf(__uint_as_float(q[0]), __uint_as_float(q[1])) : __uint_as_float(q[0]) + __uint_as_float(q[1]);
    auto z = __builtin_amdgcn_permlane16_swap(__float_as_uint(x), __float_as_uint(x), false, false);
    A = __uint_as_float(z[0]); B = __uint_as_float(z[1]);
}
DI void dual_softmax2(const f32x4 (&p)[2], const float (&lr)[2], u32x4 (&r)[2]) {
    float t[2][4], A[2], B[2];
    for (int h = 0; h < 2; ++h) {
        for (int i = 0; i < 4; ++i) t[h][i] = p[h][i] * lr[h];
        A[h] = fmaxf(fmaxf(t[h][0], t[h][1]), fmaxf(t[h][2], t[h][3]));
        B[h] = fmaxf(fmaxf(-t[h][0], -t[h][1]), fmaxf(-t[h][2], -t[h][3]));
    }
    pair_reduce<true>(A[0], B[0]); pair_reduce<true>(A[1], B[1]);
    float ep[2][4], en[2][4], sp[2], sn[2];
    for (int h = 0; h < 2; ++h) {
        for (int i = 0; i < 4; ++i) { ep[h][i] = fexp2(t[h][i] - A[h]); en[h][i] = fexp2(-t[h][i] - B[h]); }
        sp[h] = (ep[h][0] + ep[h][1]) + (ep[h][2] + ep[h][3]);
        sn[h] = (en[h][0] + en[h][1]) + (en[h][2] + en[h][3]);
    }
    pair_reduce<false>(sp[0], sn[0]); pair_reduce<false>(sp[1], sn[1]);
    for (int h = 0; h < 2; ++h) {
        float rp = frcp(sp[h]), rn = frcp(sn[h]);
        r[h].x = pk(ep[h][0] * rp, ep[h][1] * rp); r[h].y = pk(ep[h][2] * rp, ep[h][3] * rp);
        r[h].z = pk(en[h][0] * rn, en[h][1] * rn); r[h].w = pk(en[h][2] * rn, en[h][3] * rn);
    }
}

#define STAMP(i) do {} while (0)
#define STAMPR(i) do {} while (0)

constexpr int NCD = 36;
constexpr bool h_iss(int s) { return s >= 1 && s + 5 < NCD; }
constexpr bool w_iss(int s) { return s >= 1 && s + 9 < NCD; }
constexpr int C_POLLK = 3;
constexpr int C_PART  = 7;
constexpr int C_RED   = 10;
constexpr int C_RED2  = 11;
constexpr int C_O     = 12;
constexpr int C_FO    = 16;
constexpr int C_POLLO = 18;
constexpr int C_XR = 30;
constexpr int extra_ops(int u) { return (u == C_PART ? 5 : 0) + (u == C_O ? 1 : 0) + (u == C_XR ? 1 : 0); }
constexpr int n_after_h(int s) {
    int n = 0;
    if (s >= 6) {
        int u = s - 5;
        n += (w_iss(u) ? 1 : 0) + extra_ops(u);
        for (int v = u + 1; v < s; ++v) n += (h_iss(v) ? 1 : 0) + (w_iss(v) ? 1 : 0) + extra_ops(v);
    } else {
        n = (s < 4 ? 3 - s : 5 - s);
        if (s >= 1 && s < 4) n += 2;
        for (int v = 1; v < s; ++v) n += (h_iss(v) ? 1 : 0) + (w_iss(v) ? 1 : 0) + extra_ops(v);
    }
    return n;
}
constexpr int cmin(int a, int b) { return a < b ? a : b; }

__global__ __launch_bounds__(512, 2) void fused_k(const u16* __restrict__ x, const u16* __restrict__ wqkv, const u16* __restrict__ wphi,
                                                  const u16* __restrict__ wo, const u16* __restrict__ w1, const u16* __restrict__ w2,
                                                  u16* __restrict__ out, char* __restrict__ ws) {
    __shared__ __attribute__((aligned(1024))) char smem[163840];
    asm volatile("" ::"v"((unsigned)(size_t)smem));
    const int tid = threadIdx.x;
    const int w = __builtin_amdgcn_readfirstlane(tid >> 6);
    int l = tid & 63;
    const int b = blockIdx.x;
    const int xg = b & 7, rr = b >> 3;
    const int tt = (xg >> 2) * 8 + (rr & 7), j = (xg & 3) * 4 + (rr >> 3);
    const int ti = (xg >> 1) * 8 + (rr & 7), tj = (xg & 1) * 4 + (rr >> 3);
    const int tg = w & 3, cg = w >> 2;
    STAMPR(30); STAMP(0);

    u32x4 sX = make_srd(x, S * D * 2), sW1 = make_srd(w1, D * FF * 2);
    u32x4 sWS = make_srd(ws, WS_END);
    {
        u64 pc; unsigned clen;
        asm volatile("s_getpc_b64 %0\n.Lhk_code_a:\n\ts_mov_b32 %1, .Lhk_code_end-.Lhk_code_a" : "=s"(pc), "=s"(clen));
        u32x4 sC = make_srd((const void*)pc, clen);
        asm volatile("s_mov_b32 m0, %0\n\ts_nop 0\n\tbuffer_load_dword %1, %2, %3 offen lds" ::"s"(L_JUNK), "v"(l * 128), "s"(sC), "s"(w * 8192) : "memory");
    }
    const unsigned lw2k = w * 2048, lw1k = w * 1024, lw1792 = w * 1792;
    const unsigned bx = tt * 131072, bw1 = j * 224;
    const unsigned vx0 = (16 * w + (l >> 3)) * 1024 + 16 * ((l & 7) ^ (l >> 4));
    const unsigned vx1 = (16 * w + 8 + (l >> 3)) * 1024 + 16 * ((l & 7) ^ (4 + (l >> 4)));
    const unsigned vw1 = (32 * (w >> 2) + 16 * ((w >> 1) & 1) + 4 * (w & 1) + ((l * 37) >> 9)) * 3584 + 16 * (l - 14 * ((l * 37) >> 9));
    const int rot = 2 * (xg & 3) + (xg >> 2);
    auto chunk = [&](int s) { return (unsigned)((s + rot) & 7); };
    auto issue_x0 = [&](auto s, auto slot) { dmai<slot * SLOT, 0>(sX, vx0, bx + chunk(s) * 128, lw2k); };
    auto issue_x1 = [&](auto s, auto slot) { dmai<slot * SLOT + 1024, 0>(sX, vx1, bx + chunk(s) * 128, lw2k); };
    auto issue_w10 = [&](auto s, auto slot) { dmai56<slot * SLOT, 0>(sW1, vw1, bw1 + chunk(s) * (64 * 3584), lw1792); };
    auto issue_w11 = [&](auto s, auto slot) { dmai56<slot * SLOT + 896, 8 * 3584>(sW1, vw1, bw1 + chunk(s) * (64 * 3584), lw1792); };
#define IC(v) std::integral_constant<int, (v)>{}
    issue_x0(IC(0), IC(4)); issue_w10(IC(0), IC(0)); issue_x1(IC(0), IC(4)); issue_w11(IC(0), IC(0));
    issue_x0(IC(1), IC(5)); issue_w10(IC(1), IC(1)); issue_x1(IC(1), IC(5)); issue_w11(IC(1), IC(1));
    u32x4 sQ = make_srd(wqkv, D * 1536 * 2), sW2 = make_srd(w2, FF * D * 2), sWo = make_srd(wo, D * D * 2), sOut = make_srd(out, S * D * 2);
    fence_s(sQ); fence_s(sW2); fence_s(sWo); fence_s(sOut); fence_s(sWS);
    u32x2 eword = {0u, 0u};
    if (w == 0) load8_sc1(eword, sWS, 0, WS_E);
    if (w == 1) dma(make_srd(wphi, 32 * 256 * 2), l * 16, j * 1024, L_WPHI);
    if (w == 6) { if (l < 48) touch_b(sQ, (b * 48 + l) * 128, 0); }

    int g = l >> 4, l15 = l & 15, q4 = l15 >> 2, p4 = l & 3;
    const unsigned vq = (8 * ((l >> 4) & 1) + 4 * ((l >> 5) & 1) + ((l >> 2) & 3)) * 3072 + ((((l & 3) >> 1) ^ ((l >> 4) & 1)) * 32) + 16 * (l & 1);
    const unsigned bkv = j * 64 + 16 * (w & 3) * 3072, bq = j * 64 + 32 * (w & 1) * 3072;
    auto issue_kv0 = [&](auto i, auto slot) { dmai<slot * SLOT, 1024>(sQ, vq, bkv + chunk(2 * i + (w >> 2)) * (64 * 3072), lw1k); };
    auto issue_kv1 = [&](auto i, auto slot) { dmai<slot * SLOT + 8192, 2048>(sQ, vq, bkv + chunk(2 * i + (w >> 2)) * (64 * 3072), lw1k); };
    auto issue_q0 = [&](auto i, auto slot) { dmai<slot * SLOT, 0>(sQ, vq, bq + chunk(4 * i + (w >> 1)) * (64 * 3072), lw2k); };
    auto issue_q1 = [&](auto i, auto slot) { dmai<slot * SLOT + 1024, 16 * 3072>(sQ, vq, bq + chunk(4 * i + (w >> 1)) * (64 * 3072), lw2k); };

    bf16x8 ONESF = __builtin_bit_cast(bf16x8, u32x4{ONES, ONES, ONES, ONES});
    fence(ONESF);
    bf16x8 xr[2][16];
    f32x4 acc1[4][2], csacc = {0, 0, 0, 0}, gram = {0, 0, 0, 0};
    for (int i = 0; i < 4; ++i) for (int t = 0; t < 2; ++t) acc1[i][t] = f32x4{0, 0, 0, 0};
    int pb[4];
    for (int i = 0; i < 4; ++i) pb[i] = cg ? 4 + ((i + tg) % 3) : ((i + tg) & 3);
    unsigned ax[2][2];
    ax[0][0] = 65536 + (32 * tg + 16 * cg + l15) * 128 + 16 * (g ^ (l15 >> 1)); ax[0][1] = ax[0][0] ^ 64;
    ax[1][0] = 65536 + (32 * tg + 16 * (cg ^ 1) + l15) * 128 + 16 * (g ^ (l15 >> 1)); ax[1][1] = ax[1][0] ^ 64;
    unsigned aw1b[4];
    for (int i = 0; i < 4; ++i) aw1b[i] = (16 * (g >> 1) + 4 * (g & 1) + q4) * 224 + 8 * p4 + pb[i] * 32;
    fence(ax[0][0]); fence(ax[0][1]); fence(ax[1][0]); fence(ax[1][1]);
    bf16x8 f0[4], f1[4];
    f0[3] = ONESF; f1[3] = ONESF;

    auto rdA = [&](bf16x8 (&f)[4], auto s_, auto ks_, auto part_) {
        constexpr int s = s_, ks = ks_, part = part_;
        constexpr unsigned xs = (s % 4) * SLOT, wsl = (s % 4) * SLOT + 32 * ks * 224;
        if constexpr (part == 0) { xr[0][2 * s + ks] = lds_b128(ax[0][ks] + xs); f[0] = lds_tr2(aw1b[0] + wsl, aw1b[0] + wsl + 1792); }
        if constexpr (part == 1) { xr[1][2 * s + ks] = lds_b128(ax[1][ks] + xs); }
        if constexpr (part == 2) { f[1] = lds_tr2(aw1b[1] + wsl, aw1b[1] + wsl + 1792); }
        if constexpr (part == 3) { f[2] = lds_tr2(aw1b[2] + wsl, aw1b[2] + wsl + 1792); }
        if constexpr (part == 4) { if (cg == 0) f[3] = lds_tr2(aw1b[3] + wsl, aw1b[3] + wsl + 1792); }
    };
    auto groupA = [&](bf16x8 (&f)[4], auto kap_, auto&& rd, auto&& d0, auto&& d1, auto&& d2, auto&& d3) {
        constexpr int kap = kap_;
        acc1[0][0] = mfma16(f[0], xr[0][kap], acc1[0][0]); SB(); rd(IC(0)); SB(); acc1[0][1] = mfma16(f[0], xr[1][kap], acc1[0][1]); SB(); d0(); SB();
        acc1[1][0] = mfma16(f[1], xr[0][kap], acc1[1][0]); SB(); rd(IC(1)); SB(); acc1[1][1] = mfma16(f[1], xr[1][kap], acc1[1][1]); SB(); d1(); SB();
        acc1[2][0] = mfma16(f[2], xr[0][kap], acc1[2][0]); SB(); rd(IC(2)); SB(); acc1[2][1] = mfma16(f[2], xr[1][kap], acc1[2][1]); SB(); d2(); SB();
        acc1[3][0] = mfma16(f[3], xr[0][kap], acc1[3][0]); SB(); rd(IC(3)); SB(); acc1[3][1] = mfma16(f[3], xr[1][kap], acc1[3][1]); SB(); d3(); SB();
        csacc = mfma16(f[0], ONESF, csacc); SB(); rd(IC(4)); SB();
        gram = mfma16(xr[0][kap], xr[0][kap], gram); SB();
    };
    auto nop = [&]() {};
    auto nord = [&](auto) {};
    sfor<0, 8>([&](auto s_) {
        constexpr int s = s_;
        sync<4, s>();
        if constexpr (s == 0) STAMP(1);
        if constexpr (s == 4) STAMP(2);
        auto dA = [&]() {
            if constexpr (s <= 5) issue_x0(IC(s + 2), IC(4 + (s + 2) % 4));
            else if constexpr (s == 6) issue_kv0(IC(0), IC(4));
            else issue_kv0(IC(1), IC(5));
        };
        auto dB = [&]() {
            if constexpr (s <= 5) issue_w10(IC(s + 2), IC((s + 2) % 4));
            else if constexpr (s == 6) issue_kv1(IC(0), IC(4));
            else issue_kv1(IC(1), IC(5));
        };
        auto dC = [&]() {
            if constexpr (s <= 5) issue_x1(IC(s + 2), IC(4 + (s + 2) % 4));
            else if constexpr (s == 6) issue_kv0(IC(3), IC(0));
            else issue_q0(IC(0), IC(1));
        };
        auto dD = [&]() {
            if constexpr (s <= 5) issue_w11(IC(s + 2), IC((s + 2) % 4));
            else if constexpr (s == 6) issue_kv1(IC(3), IC(0));
            else issue_q1(IC(0), IC(1));
        };
        auto r0 = [&](auto p) { rdA(f0, IC(s), IC(0), p); };
        auto r1 = [&](auto p) { rdA(f1, IC(s), IC(1), p); };
        if constexpr (s > 0) {
            groupA(f1, IC(2 * s - 1), r0, dA, nop, dB, nop);
            groupA(f0, IC(2 * s), r1, dC, nop, dD, nop);
        } else {
            r0(IC(0)); r0(IC(1)); r0(IC(2)); r0(IC(3)); r0(IC(4)); SB();
            groupA(f0, IC(0), r1, dA, dB, dC, dD);
        }
    });
    groupA(f1, IC(15), nord, nop, nop, nop, nop);
    {
        float dg = (l15 & 2) ? ((l15 & 1) ? gram[3] : gram[2]) : ((l15 & 1) ? gram[1] : gram[0]);
        if (g == (l15 >> 2)) *(LAS float*)(L_STATS + (32 * tg + 16 * cg + l15) * 8 + 4) = dg;
        if (g == 0 && cg) {
            *(LAS float*)(L_STATS + (32 * tg + 16 + l15) * 8) = acc1[3][0][0];
            *(LAS float*)(L_STATS + (32 * tg + l15) * 8) = acc1[3][1][0];
        }
        if (l15 == 0) *(LAS f32x4*)(L_CS + (pb[0] * 16 + 4 * g) * 4) = csacc;
    }
    bar();
    STAMP(3);
    fence(l); g = l >> 4; l15 = l & 15; q4 = l15 >> 2; p4 = l & 3;
    const unsigned vw2 = (8 * w + (l >> 3)) * 1024 + 16 * ((l & 7) ^ (4 * ((l >> 4) & 1)));
    const unsigned vh = l * 16;
    const unsigned bw2 = tj * 128;
    const unsigned bh = WS_H + ((2 * ti + (w >> 2)) * HSTR + (w & 3)) * 1024, bo = WS_O + ((2 * ti + (w >> 2)) * OSTR + (w & 3)) * 1024;
    auto issue_W = [&](auto i_) {
        constexpr int i = i_;
        constexpr unsigned dst = 3 * SLOT + ((i + 8) % 10) * 8192;
        if constexpr (i < 28) dmai<dst, i * 65536>(sW2, vw2, bw2, lw1k); else dmai<dst, (i - 28) * 65536>(sWo, vw2, bw2, lw1k);
    };
    auto issue_H = [&](auto i_) {
        constexpr int i = i_;
        constexpr unsigned dst = (i % 6) * 8192;
        if constexpr (i < 28) dmai<dst, i * 4096, 1>(sWS, vh, bh, lw1k); else dmai<dst, (i - 28) * 4096, 1>(sWS, vh, bo, lw1k);
    };
    issue_kv0(IC(2), IC(6)); issue_kv1(IC(2), IC(6)); issue_q0(IC(1), IC(2)); issue_q1(IC(1), IC(2));
    float nm[2], sdv[2], rsv[2];
    for (int th = 0; th < 2; ++th) {
        f32x2 o = *(const LAS f32x2*)(L_STATS + (32 * tg + 16 * (th ^ cg) + l15) * 8);
        float mean = o[0] * (1.f / 512.f);
        float var = fmaxf(o[1] * (1.f / 512.f) - mean * mean, 0.f);
        nm[th] = -rbf(mean);
        sdv[th] = rbf(__builtin_amdgcn_sqrtf(rbf(rbf(var) + 1e-5f)));
        rsv[th] = frcp(sdv[th]);
    }
    sfor<0, 4>([&](auto i_) {
        constexpr int i = i_;
        if (i < 3 || cg == 0) {
            f32x4 c = lds_f4(L_CS + (pb[i] * 16 + 4 * g) * 4);
            unsigned pa[2][2];
            for (int th = 0; th < 2; ++th) {
                float hv[4], rl = rsv[th] * -LOG2E;
                for (int r = 0; r < 4; ++r) {
                    float u = __builtin_fmaf(nm[th], c[r], acc1[i][th][r]);
                    float e = fexp2(u * rl);
                    hv[r] = u * frcp(__builtin_fmaf(e, sdv[th], sdv[th]));
                }
                pa[th][0] = pk(hv[0], hv[1]); pa[th][1] = pk(hv[2], hv[3]);
            }
            auto r0 = __builtin_amdgcn_permlane16_swap(pa[0][0], pa[1][0], false, false);
            auto r1 = __builtin_amdgcn_permlane16_swap(pa[0][1], pa[1][1], false, false);
            u32x4 v = {r0[0], r1[0], r0[1], r1[1]};
            store16_sc1(sWS, 16 * (l ^ (16 * cg)), WS_H + ((4 * tt + tg) * HSTR + 7 * j + pb[i]) * 1024, v);
        }
        SB();
    });
    if (w == 7) { if (l < 56) touch_b(sW2, (b * 56 + l) * 128, 0); }
    if (w == 6) { if (l < 16) touch_b(sWo, (b * 16 + l) * 128, 0); }

    f32x4 accK[2], accV[2], accQ[2], csb = {0, 0, 0, 0}, csK = {0, 0, 0, 0}, csV = {0, 0, 0, 0};
    for (int t = 0; t < 2; ++t) { accK[t] = f32x4{0, 0, 0, 0}; accV[t] = accK[t]; accQ[t] = accK[t]; }
    const unsigned rowb = 16 * (g >> 1) + 4 * (g & 1) + q4;
    unsigned aq = rowb * 64 + 32 * (cg ^ (g & 1)) + 8 * p4;
    unsigned aqh = aq + 65536;
    fence(aqh);
    const int roleKV = (tg == cg) ? 1 : ((tg == cg + 2) ? 2 : 0);
    const bool roleQ = (tg == 1 - cg);
    unsigned tlo = 0, thi = 0;
    u32x2 pollv = {0u, 0u};
    bf16x8 fa[4], fb[4];

    auto rdKV = [&](bf16x8 (&f)[4], auto i_, auto hf_, auto part_) {
        constexpr int i = i_, hf = hf_, part = part_;
        constexpr int slot = (i == 3) ? 0 : 4 + i;
        constexpr unsigned o = (slot >= 4 ? slot - 4 : slot) * SLOT + (64 * hf + 32 * (part >> 1)) * 64 + (part & 1) * 8192;
        const unsigned bb = slot >= 4 ? aqh : aq;
        f[part] = lds_tr2(bb + o, bb + o + 512);
    };
    auto groupKV = [&](bf16x8 (&f)[4], auto kap_, auto&& rd, auto&& d0, auto&& d1) {
        constexpr int kap = kap_;
        accK[0] = mfma16(f[0], xr[0][kap], accK[0]); SB(); rd(IC(0)); SB(); accK[1] = mfma16(f[0], xr[1][kap], accK[1]); SB(); rd(IC(1)); SB();
        accV[0] = mfma16(f[1], xr[0][kap], accV[0]); SB(); rd(IC(2)); SB(); accV[1] = mfma16(f[1], xr[1][kap], accV[1]); SB(); rd(IC(3)); SB();
        accK[0] = mfma16(f[2], xr[0][kap + 1], accK[0]); SB(); d0(); SB(); accK[1] = mfma16(f[2], xr[1][kap + 1], accK[1]); SB();
        accV[0] = mfma16(f[3], xr[0][kap + 1], accV[0]); SB(); d1(); SB(); accV[1] = mfma16(f[3], xr[1][kap + 1], accV[1]); SB();
        if (roleKV == 1) { csK = mfma16(f[0], ONESF, csK); csK = mfma16(f[2], ONESF, csK); }
        if (roleKV == 2) { csV = mfma16(f[1], ONESF, csV); csV = mfma16(f[3], ONESF, csV); }
        SB();
    };
    sfor<0, 4>([&](auto i_) {
        constexpr int i = i_;
        if constexpr (i == 0) { sync<13, 8>(); STAMP(4); }
        if constexpr (i == 1) sync<9, 9>();
        if constexpr (i == 2) sync<9, 10>();
        if constexpr (i == 3) sync<63, 11>();
        auto d0 = [&]() { if constexpr (i == 1) issue_W(IC(0)); if constexpr (i == 2) issue_W(IC(4)); if constexpr (i == 3) issue_W(IC(8)); };
        auto d1 = [&]() { if constexpr (i == 1) issue_W(IC(1)); if constexpr (i == 2) issue_W(IC(5)); if constexpr (i == 3) issue_W(IC(9)); };
        auto d2 = [&]() { if constexpr (i == 1) issue_W(IC(2)); if constexpr (i == 2) issue_W(IC(6)); };
        auto d3 = [&]() { if constexpr (i == 1) issue_W(IC(3)); if constexpr (i == 2) issue_W(IC(7)); };
        auto ra = [&](auto p) { rdKV(fa, IC(i), IC(0), p); };
        auto rb = [&](auto p) { rdKV(fb, IC(i), IC(1), p); };
        if constexpr (i > 0) {
            groupKV(fb, IC(4 * i - 2), ra, d0, d1);
        } else { ra(IC(0)); ra(IC(1)); ra(IC(2)); ra(IC(3)); SB(); d0(); d1(); SB(); }
        groupKV(fa, IC(4 * i), rb, d2, d3);
    });
    groupKV(fb, IC(14), nord, nop, nop);
    if (l15 == 0) {
        if (roleKV == 1) *(LAS f32x4*)(L_CS + ((7 + cg) * 16 + 4 * g) * 4) = csK;
        if (roleKV == 2) *(LAS f32x4*)(L_CS + ((9 + cg) * 16 + 4 * g) * 4) = csV;
    }
    waitv<10>();
    bar();
    STAMP(5);
    if (w == 0) {
        fence(eword);
        u64 e = ((u64)__builtin_amdgcn_readfirstlane(eword.y) << 32) | __builtin_amdgcn_readfirstlane(eword.x);
        e += 0x9E3779B97F4A7C15ull;
        tlo = (unsigned)e; thi = (unsigned)(e >> 32);
        if (l == 0) store8_sc1(sWS, 0, WS_FH + (tt * 16 + j) * 8, u32x2{tlo, thi});
    }
    const s16x4 wph = lds_tr(L_WPHI + cg * 512 + (4 * g + q4) * 32 + 8 * p4);
    float lr[2] = {rsv[0] * LOG2E, rsv[1] * LOG2E};
    {
        f32x4 cK = lds_f4(L_CS + ((7 + cg) * 16 + 4 * g) * 4), cV = lds_f4(L_CS + ((9 + cg) * 16 + 4 * g) * 4);
        f32x4 pkv[2];
        for (int th = 0; th < 2; ++th) {
            float k0 = __builtin_fmaf(nm[th], cK[0], accK[th][0]), k1 = __builtin_fmaf(nm[th], cK[1], accK[th][1]);
            float k2 = __builtin_fmaf(nm[th], cK[2], accK[th][2]), k3 = __builtin_fmaf(nm[th], cK[3], accK[th][3]);
            u32x2 kb = {pk(k0, k1), pk(k2, k3)};
            pkv[th] = __builtin_amdgcn_mfma_f32_16x16x16bf16_1k(wph, __builtin_bit_cast(s16x4, kb), f32x4{0, 0, 0, 0}, 0, 0, 0);
        }
        for (int th = 0; th < 2; ++th) {
            float v0 = __builtin_fmaf(nm[th], cV[0], accV[th][0]) * rsv[th], v1 = __builtin_fmaf(nm[th], cV[1], accV[th][1]) * rsv[th];
            float v2 = __builtin_fmaf(nm[th], cV[2], accV[th][2]) * rsv[th], v3 = __builtin_fmaf(nm[th], cV[3], accV[th][3]) * rsv[th];
            unsigned row = L_T + cg * 12288 + (32 * tg + 16 * th + l15) * 96 + 8 * g;
            *(LAS u32x2*)(row + 64) = u32x2{pk(v0, v1), pk(v2, v3)};
        }
        u32x4 ph[2];
        dual_softmax2(pkv, lr, ph);
        for (int th = 0; th < 2; ++th) {
            unsigned row = L_T + cg * 12288 + (32 * tg + 16 * th + l15) * 96 + 8 * g;
            *(LAS u32x2*)(row) = u32x2{ph[th].x, ph[th].y};
            *(LAS u32x2*)(row + 32) = u32x2{ph[th].z, ph[th].w};
        }
    }
    bar();
    STAMP(6);
    if (w == 0) load8_sc1(pollv, sWS, l15 * 8, WS_FH + (ti >> 1) * 128);
    if (tg < 2) {
        f32x4 akv = {0, 0, 0, 0}, az = {0, 0, 0, 0};
        const unsigned base = L_T + cg * 12288 + rowb * 96 + 8 * p4;
        bf16x8 a[4], v[4];
        for (int kk = 0; kk < 4; ++kk) {
            a[kk] = lds_tr2(base + (32 * kk) * 96 + tg * 32, base + (32 * kk + 8) * 96 + tg * 32);
            v[kk] = lds_tr2(base + (32 * kk) * 96 + 64, base + (32 * kk + 8) * 96 + 64);
        }
        for (int kk = 0; kk < 4; ++kk) { akv = mfma16(a[kk], v[kk], akv); az = mfma16(a[kk], ONESF, az); }
        const unsigned pidx = __builtin_amdgcn_readfirstlane((j * 16 + tt) * 2 + cg);
        const unsigned so_z = __builtin_amdgcn_readfirstlane(WS_ZP + pidx * 128), so_kv = __builtin_amdgcn_readfirstlane(WS_KVP + pidx * 1024);
        store8_sc1_v(sWS, l * 16 + tg * 8, so_kv, u32x2{pk(akv[0], akv[1]), pk(akv[2], akv[3])});
        if (l15 == 0) store16_sc1_m(sWS, g * 32 + tg * 16, so_z, az);
    }
    csb = f32x4{0, 0, 0, 0};
    auto rdQ = [&](bf16x8 (&f)[4], auto i_, auto hf_, auto part_) {
        constexpr int i = i_, hf = hf_, part = part_;
        constexpr unsigned o = (1 + i) * SLOT + (128 * hf + 32 * part) * 64;
        f[part] = lds_tr2(aq + o, aq + o + 512);
    };
    auto groupQ = [&](bf16x8 (&f)[4], auto kap_, auto&& rd, auto&& d0, auto&& d1) {
        constexpr int kap = kap_;
        accQ[0] = mfma16(f[0], xr[0][kap], accQ[0]); SB(); rd(IC(0)); SB(); accQ[1] = mfma16(f[0], xr[1][kap], accQ[1]); SB(); rd(IC(1)); SB();
        accQ[0] = mfma16(f[1], xr[0][kap + 1], accQ[0]); SB(); rd(IC(2)); SB(); accQ[1] = mfma16(f[1], xr[1][kap + 1], accQ[1]); SB(); rd(IC(3)); SB();
        accQ[0] = mfma16(f[2], xr[0][kap + 2], accQ[0]); SB(); d0(); SB(); accQ[1] = mfma16(f[2], xr[1][kap + 2], accQ[1]); SB();
        accQ[0] = mfma16(f[3], xr[0][kap + 3], accQ[0]); SB(); d1(); SB(); accQ[1] = mfma16(f[3], xr[1][kap + 3], accQ[1]); SB();
        if (roleQ) { csb = mfma16(f[0], ONESF, csb); csb = mfma16(f[1], ONESF, csb); csb = mfma16(f[2], ONESF, csb); csb = mfma16(f[3], ONESF, csb); }
        SB();
    };
    sfor<0, 2>([&](auto i_) {
        constexpr int i = i_;
        if constexpr (i == 1) {
            if (w == 0) {
                waitv<2>();
                fence(pollv);
                unsigned spins = 0;
                while (!__all(pollv.x == tlo && pollv.y == thi)) {
                    __builtin_amdgcn_s_sleep(1);
                    load8_sc1(pollv, sWS, l15 * 8, WS_FH + (ti >> 1) * 128);
                    waitv<0>();
                    fence(pollv);
                    if (++spins > (1u << 16)) { *(volatile unsigned*)(ws + WS_TMO) = 1; break; }
                }
            }
            sync<63, 13>();
        } else { sync<63, 12>(); STAMP(7); }
        auto d0 = [&]() { if constexpr (i == 1) { issue_H(IC(0)); issue_H(IC(1)); } };
        auto d1 = [&]() { if constexpr (i == 1) { issue_H(IC(2)); issue_H(IC(3)); } };
        auto ra = [&](auto p) { rdQ(fa, IC(i), IC(0), p); };
        auto rb = [&](auto p) { rdQ(fb, IC(i), IC(1), p); };
        if constexpr (i > 0) groupQ(fb, IC(4), ra, nop, nop);
        else { ra(IC(0)); ra(IC(1)); ra(IC(2)); ra(IC(3)); SB(); }
        groupQ(fa, IC(8 * i), rb, d0, d1);
    });
    groupQ(fb, IC(12), nord, nop, nop);
    if (roleQ && l15 == 0) *(LAS f32x4*)(L_CS + ((11 + cg) * 16 + 4 * g) * 4) = csb;
    bf16x8 phq[2];
    f32x4 pq[2]; float tq[2][4], Aq[2], Bq[2], epq[2][4], enq[2][4], spq[2], snq[2];
    auto aq_step = [&](auto k_) {
        constexpr int k = k_;
        if constexpr (k == 0) {
            f32x4 cQ = lds_f4(L_CS + ((11 + cg) * 16 + 4 * g) * 4);
            for (int th = 0; th < 2; ++th) {
                float k0 = __builtin_fmaf(nm[th], cQ[0], accQ[th][0]), k1 = __builtin_fmaf(nm[th], cQ[1], accQ[th][1]);
                float k2 = __builtin_fmaf(nm[th], cQ[2], accQ[th][2]), k3 = __builtin_fmaf(nm[th], cQ[3], accQ[th][3]);
                u32x2 kb = {pk(k0, k1), pk(k2, k3)};
                pq[th] = __builtin_amdgcn_mfma_f32_16x16x16bf16_1k(wph, __builtin_bit_cast(s16x4, kb), f32x4{0, 0, 0, 0}, 0, 0, 0);
                fence(pq[th]);
            }
        }
        if constexpr (k == 1) {
            for (int h = 0; h < 2; ++h) {
                for (int i = 0; i < 4; ++i) tq[h][i] = pq[h][i] * lr[h];
                Aq[h] = fmaxf(fmaxf(tq[h][0], tq[h][1]), fmaxf(tq[h][2], tq[h][3]));
                Bq[h] = fmaxf(fmaxf(-tq[h][0], -tq[h][1]), fmaxf(-tq[h][2], -tq[h][3]));
            }
            pair_reduce<true>(Aq[0], Bq[0]); pair_reduce<true>(Aq[1], Bq[1]);
            for (int h = 0; h < 2; ++h) { fence(Aq[h]); fence(Bq[h]); for (int i = 0; i < 4; ++i) fence(tq[h][i]); }
        }
        if constexpr (k == 2 || k == 3) {
            constexpr int h = k - 2;
            for (int i = 0; i < 4; ++i) { epq[h][i] = fexp2(tq[h][i] - Aq[h]); enq[h][i] = fexp2(-tq[h][i] - Bq[h]); }
            spq[h] = (epq[h][0] + epq[h][1]) + (epq[h][2] + epq[h][3]);
            snq[h] = (enq[h][0] + enq[h][1]) + (enq[h][2] + enq[h][3]);
            fence(spq[h]); fence(snq[h]);
            for (int i = 0; i < 4; ++i) { fence(epq[h][i]); fence(enq[h][i]); }
        }
        if constexpr (k == 4) {
            pair_reduce<false>(spq[0], snq[0]); pair_reduce<false>(spq[1], snq[1]);
            for (int h = 0; h < 2; ++h) { spq[h] = frcp(spq[h]); snq[h] = frcp(snq[h]); fence(spq[h]); fence(snq[h]); }
        }
        if constexpr (k == 5) {
            for (int h = 0; h < 2; ++h) {
                u32x4 r;
                r.x = pk(epq[h][0] * spq[h], epq[h][1] * spq[h]); r.y = pk(epq[h][2] * spq[h], epq[h][3] * spq[h]);
                r.z = pk(enq[h][0] * snq[h], enq[h][1] * snq[h]); r.w = pk(enq[h][2] * snq[h], enq[h][3] * snq[h]);
                phq[h] = __builtin_bit_cast(bf16x8, r);
                fence(phq[h]);
            }
        }
    };

    const int nbk = w & 1, uu = (w >> 1) & 1, kh = w >> 2;
    f32x16 accO, accS;
    for (int i = 0; i < 16; ++i) { accO[i] = 0.f; accS[i] = 0.f; }
    const unsigned h2 = l >> 5, blk = (l >> 4) & 1;
    unsigned aW = (8 * h2 + q4 + 32 * kh) * 128 + 64 * (nbk ^ (q4 >> 1)) + 32 * blk + 8 * p4;
    unsigned aWh = aW + 65536;
    fence(aWh);
    const unsigned aH = (uu * 4 + 2 * kh) * 1024 + 16 * l;
    u32x4 part[5];
    u32x4 xres;
    bf16x8 hb[2][2], wa[2][2];

    auto mfmaC = [&](auto s_) {
        constexpr int s = s_;
        if constexpr (s < 28) { accO = mfma32(wa[s & 1][0], hb[s & 1][0], accO); accO = mfma32(wa[s & 1][1], hb[s & 1][1], accO); }
        else { accS = mfma32(wa[s & 1][0], hb[s & 1][0], accS); accS = mfma32(wa[s & 1][1], hb[s & 1][1], accS); }
    };
    sfor<0, NCD>([&](auto s_) {
        constexpr int s = s_;
        if constexpr (s == C_PART || s == 23) {
            if (w == 0) {
                constexpr unsigned fam = (s == C_PART) ? WS_FK : WS_FO;
                const unsigned fo = fam + (s == C_PART ? j : (ti >> 1)) * 128;
                waitv<2 * (s - (s == C_PART ? C_POLLK : C_POLLO))>();
                fence(pollv);
                unsigned spins = 0;
                while (!__all(pollv.x == tlo && pollv.y == thi)) {
                    __builtin_amdgcn_s_sleep(1);
                    load8_sc1(pollv, sWS, l15 * 8, fo);
                    waitv<0>();
                    fence(pollv);
                    if (++spins > (1u << 16)) { *(volatile unsigned*)(ws + WS_TMO) = 2; break; }
                }
            }
        }
        if constexpr (s == 0) sync<0, 14>();
        else if constexpr (s == C_FO) sync<cmin(2 * (C_FO - C_O - 1), n_after_h(s)), 14 + s>();
        else if constexpr (s == C_RED) sync<cmin(2 * (C_RED - C_PART - 1), n_after_h(s)), 14 + s>();
        else sync<n_after_h(s), 14 + s>();
        if constexpr (s == 0) STAMP(8);
        if constexpr (s == 6) STAMP(9);
        if constexpr (s == C_PART) STAMP(10);
        if constexpr (s == C_O + 1) STAMP(11);
        if constexpr (s == 22) STAMP(12);
        if constexpr (s == 24) STAMP(13);
        if constexpr (s == 28) STAMP(14);
        {
            constexpr unsigned hs = (s % 6) * 8192, wo_ = 3 * SLOT + ((s + 8) % 10) * 8192;
            constexpr bool hi = wo_ >= 65536;
            constexpr unsigned wof = hi ? wo_ - 65536 : wo_;
            const unsigned bw = hi ? aWh : aW;
            hb[s & 1][0] = lds_b128(aH + hs);
            wa[s & 1][0] = lds_tr2(bw + wof, bw + wof + 512);
            hb[s & 1][1] = lds_b128(aH + hs + 1024);
            wa[s & 1][1] = lds_tr2(bw + wof + 2048, bw + wof + 2048 + 512);
        }
        SB();
        if constexpr (s == 0) { if (w == 0 && l == 0) store8_sc1(sWS, 0, WS_FK + (j * 16 + tt) * 8, u32x2{tlo, thi}); }
        if constexpr (s == C_FO) { if (w == 0 && l == 0) store8_sc1(sWS, 0, WS_FO + (tt * 16 + j) * 8, u32x2{tlo, thi}); }
        if constexpr (s == C_POLLK) { if (w == 0) load8_sc1(pollv, sWS, l15 * 8, WS_FK + j * 128); }
        if constexpr (s == C_POLLO) { if (w == 0) load8_sc1(pollv, sWS, l15 * 8, WS_FO + (ti >> 1) * 128); }
        if constexpr (s > 0) mfmaC(IC(s - 1));
        SB();
        if constexpr (s == 0) { issue_H(IC(4)); issue_H(IC(5)); }
        if constexpr (h_iss(s)) issue_H(IC(s + 5));
        if constexpr (w_iss(s)) issue_W(IC(s + 9));
        if constexpr (s <= 5) aq_step(IC(s));
        if constexpr (s == C_PART) {
            const unsigned em = (j * 16 + 4 * tg) * 2 + cg;
            sfor<0, 4>([&](auto i_) { load16_sc1(part[i_], sWS, l * 16, WS_KVP + (em + 2 * i_) * 1024); });
            load16_sc1(part[4], sWS, ((l >> 3) & 3) * 256 + (l & 7) * 16, WS_ZP + em * 128);
        }
        if constexpr (s == C_XR) load16(xres, sX, (64 * ti + 32 * uu + (l & 31)) * 1024 + (64 * tj + 32 * nbk + 16 * kh + 8 * h2) * 2, 0);
        if constexpr (s == C_RED) {
            sfor<0, 5>([&](auto i_) { fence(part[i_]); });
            const unsigned e = L_T + (cg * 4 + tg) * 2048 + l * 8;
            for (int d = 0; d < 4; ++d) {
                float lo = (lo_f(part[0][d]) + lo_f(part[1][d])) + (lo_f(part[2][d]) + lo_f(part[3][d]));
                float hi = (hi_f(part[0][d]) + hi_f(part[1][d])) + (hi_f(part[2][d]) + hi_f(part[3][d]));
                *(LAS f32x2*)(e + d * 512) = f32x2{lo, hi};
            }
            f32x4 z = __builtin_bit_cast(f32x4, part[4]);
            for (int r = 0; r < 4; ++r) {
                float a = z[r] + __uint_as_float(__builtin_amdgcn_update_dpp(0u, __float_as_uint(z[r]), 0x128  , 0xf, 0xf, false));
                auto sw = __builtin_amdgcn_permlane16_swap(__float_as_uint(a), __float_as_uint(a), false, false);
                z[r] = __uint_as_float(sw[0]) + __uint_as_float(sw[1]);
            }
            if (l < 8) *(LAS f32x4*)(L_T + 16384 + (cg * 4 + tg) * 128 + l * 16) = z;
        }
        if constexpr (s == C_RED2) {
            const unsigned e = L_T + (cg * 4) * 2048 + tg * 512 + l * 8;
            f32x2 a0 = *(const LAS f32x2*)(e), a1 = *(const LAS f32x2*)(e + 2048), a2 = *(const LAS f32x2*)(e + 4096), a3 = *(const LAS f32x2*)(e + 6144);
            f32x2 t = (a0 + a1) + (a2 + a3);
            *(LAS unsigned*)(L_T + 18432 + cg * 1024 + l * 16 + tg * 4) = pk(t[0], t[1]);
            if (tg == 0 && l < 8) {
                const unsigned ez = L_T + 16384 + cg * 512 + l * 16;
                f32x4 z = (lds_f4(ez) + lds_f4(ez + 128)) + (lds_f4(ez + 256) + lds_f4(ez + 384));
                *(LAS u32x2*)(L_T + 20480 + cg * 64 + l * 8) = u32x2{pk(z[0], z[1]), pk(z[2], z[3])};
            }
        }
        if constexpr (s == C_O) {
            bf16x8 akv = lds_b128(L_T + 18432 + cg * 1024 + l * 16);
            bf16x8 az = lds_b128(L_T + 20480 + cg * 64 + g * 16);
            unsigned po[2][2];
            for (int th = 0; th < 2; ++th) {
                f32x4 num = mfma16(akv, phq[th], f32x4{0, 0, 0, 0});
                f32x4 den = mfma16(az, phq[th], f32x4{0, 0, 0, 0});
                float rd = frcp(den[0] + 1e-6f);
                po[th][0] = pk(num[0] * rd, num[1] * rd); po[th][1] = pk(num[2] * rd, num[3] * rd);
            }
            auto r0 = __builtin_amdgcn_permlane16_swap(po[0][0], po[1][0], false, false);
            auto r1 = __builtin_amdgcn_permlane16_swap(po[0][1], po[1][1], false, false);
            u32x4 v = {r0[0], r1[0], r0[1], r1[1]};
            store16_sc1(sWS, 16 * (l ^ (16 * cg)), WS_O + ((4 * tt + tg) * OSTR + 2 * j + cg) * 1024, v);
        }
        SB();
    });
    const unsigned ooff = (64 * ti + 32 * uu + (l & 31)) * 1024 + (64 * tj + 32 * nbk + 16 * kh + 8 * h2) * 2;
    mfmaC(IC(NCD - 1));
    bar();
    STAMP(15);
    {
        unsigned e = w * 4096 + l * 16;
        f32x4 o0, o1, s0, s1;
        if (kh == 0) { o0 = f32x4{accO[8], accO[9], accO[10], accO[11]}; o1 = f32x4{accO[12], accO[13], accO[14], accO[15]};
                       s0 = f32x4{accS[8], accS[9], accS[10], accS[11]}; s1 = f32x4{accS[12], accS[13], accS[14], accS[15]}; }
        else         { o0 = f32x4{accO[0], accO[1], accO[2], accO[3]}; o1 = f32x4{accO[4], accO[5], accO[6], accO[7]};
                       s0 = f32x4{accS[0], accS[1], accS[2], accS[3]}; s1 = f32x4{accS[4], accS[5], accS[6], accS[7]}; }
        *(LAS f32x4*)(e) = o0; *(LAS f32x4*)(e + 1024) = o1; *(LAS f32x4*)(e + 2048) = s0; *(LAS f32x4*)(e + 3072) = s1;
    }
    bar();
    {
        unsigned e = (w ^ 4) * 4096 + l * 16;
        f32x4 o0 = lds_f4(e), o1 = lds_f4(e + 1024), s0 = lds_f4(e + 2048), s1 = lds_f4(e + 3072);
        float ov[8], sv[8];
        if (kh == 0) { for (int i = 0; i < 4; ++i) { ov[i] = accO[i] + o0[i]; ov[4 + i] = accO[4 + i] + o1[i]; sv[i] = accS[i] + s0[i]; sv[4 + i] = accS[4 + i] + s1[i]; } }
        else         { for (int i = 0; i < 4; ++i) { ov[i] = accO[8 + i] + o0[i]; ov[4 + i] = accO[12 + i] + o1[i]; sv[i] = accS[8 + i] + s0[i]; sv[4 + i] = accS[12 + i] + s1[i]; } }
        float gs[8];
        for (int i = 0; i < 8; ++i) gs[i] = sv[i] * ov[i] * frcp(1.f + fexp2(ov[i] * -LOG2E));
        waitv<0>();
        fence(xres);
        auto x02 = __builtin_amdgcn_permlane32_swap(xres.x, xres.z, false, false);
        auto x13 = __builtin_amdgcn_permlane32_swap(xres.y, xres.w, false, false);
        unsigned xp[4] = {x02[0], x13[0], x02[1], x13[1]};
        unsigned rp[4];
        for (int i = 0; i < 4; ++i) rp[i] = pk(lo_f(xp[i]) + gs[2 * i], hi_f(xp[i]) + gs[2 * i + 1]);
        auto r02 = __builtin_amdgcn_permlane32_swap(rp[0], rp[2], false, false);
        auto r13 = __builtin_amdgcn_permlane32_swap(rp[1], rp[3], false, false);
        u32x4 v = {r02[0], r13[0], r02[1], r13[1]};
        asm volatile("buffer_store_dwordx4 %0, %1, %2, 0 offen\n\ts_nop 1" ::"v"(v), "v"(ooff), "s"(sOut) : "memory");
    }
    if (b == 0 && w == 0 && l == 0) store8_sc1(sWS, 0, WS_E, u32x2{tlo, thi});
    asm volatile(".Lhk_code_end:");
}

}

extern "C" void kernel_launch(void* const* d_in, const int* in_sizes, int n_in, void* d_out, int out_size, void* d_ws, size_t ws_size,
                              hipStream_t stream) {
    hk::fused_k<<<256, 512, 0, stream>>>((const hk::u16*)d_in[0], (const hk::u16*)d_in[1], (const hk::u16*)d_in[2], (const hk::u16*)d_in[3],
                                         (const hk::u16*)d_in[5], (const hk::u16*)d_in[6], (hk::u16*)d_out, (char*)d_ws);
}
```
